```python
import torch

def _softmax(x, axis=-1):
    return torch.softmax(x, dim=axis)
def _gelu(x):
    return torch.nn.functional.gelu(x, approximate="tanh")
def _silu(x):
    return torch.nn.functional.silu(x)
def _sigmoid(x):
    return torch.sigmoid(x)
def _layernorm(x, axis=-1, eps=1e-5):
    m = x.mean(dim=axis, keepdim=True)
    v = x.var(dim=axis, keepdim=True, unbiased=False)
    return (x - m) / torch.sqrt(v + eps)
def _scatter_add(tgt, idx, src, axis=0):
    return tgt.clone().index_add_(axis, idx, src)
def _segment_sum(src, seg, n):
    out = torch.zeros((n,) + src.shape[1:], dtype=src.dtype)
    return out.index_add_(0, seg, src)
def _pad(x, pw):
    flat = tuple(v for pair in reversed(pw) for v in pair)
    return torch.nn.functional.pad(x, flat)

kernel_name = 'interleave_layers__sliding_block__glu_gated_geglu__prompt'

def setup_inputs():

    _w = (lambda a: (lambda t: t.to(torch.bfloat16) if t.is_floating_point() else t)(torch.as_tensor(a)))
    def _A_setup():
        B, S, D = 1, 2048, 640
        return {
            "x": _w((lambda *s: torch.randn(*s, dtype=torch.bfloat16) * 0.05)(B, S, D)),
            "wqkv": _w((lambda *s: torch.randn(*s, dtype=torch.bfloat16) * 0.05)(D, 3 * D)),
            "wo": _w((lambda *s: torch.randn(*s, dtype=torch.bfloat16) * 0.05)(D, D)),
        }
    _A_in = _A_setup()

    def _B_setup():
        B, S, Dm, Df = 1, 2048, 640, 1728
        return {
            "x": _w((lambda *s: torch.randn(*s, dtype=torch.bfloat16) * 0.05)(B, S, Dm)),
            "w_gate": _w((lambda *s: torch.randn(*s, dtype=torch.bfloat16) * 0.8)(Dm, Df) * (Dm ** -0.5)),
            "w_up": _w((lambda *s: torch.randn(*s, dtype=torch.bfloat16) * 0.8)(Dm, Df) * (Dm ** -0.5)),
            "w_down": _w((lambda *s: torch.randn(*s, dtype=torch.bfloat16) * 0.8)(Df, Dm) * (Df ** -0.5)),
        }
    _B_in = _B_setup()

    return {
        "A_x": _A_in["x"],
        "A_wqkv": _A_in["wqkv"],
        "A_wo": _A_in["wo"],
        "B_x": _B_in["x"],
        "B_w_gate": _B_in["w_gate"],
        "B_w_up": _B_in["w_up"],
        "B_w_down": _B_in["w_down"],
    }

def reference(A_x, A_wqkv, A_wo, B_x, B_w_gate, B_w_up, B_w_down):
    def _A_ref(x, wqkv, wo):
        B, S, D = x.shape
        H = 32; Dh = D // H; W = 256
        qkv = (lambda x, *d: x.permute(*d))((x @ wqkv).reshape(B, S, 3, H, Dh), 2, 0, 3, 1, 4)
        q, k, v = qkv[0], qkv[1], qkv[2]
        scores = torch.einsum("bhqd,bhkd->bhqk", q, k) * (Dh ** -0.5)
        idx = torch.arange(S)
        mask = (idx[:, None] - idx[None, :] >= 0) & (idx[:, None] - idx[None, :] < W)
        scores = torch.where(mask[None, None], scores, -1e9)
        p = _softmax(scores, axis=-1)
        o = torch.einsum("bhqk,bhkd->bhqd", p, v)
        return (lambda x, *d: x.permute(*d))(o, 0, 2, 1, 3).reshape(B, S, D) @ wo

    def _B_ref(x, w_gate, w_up, w_down):
        g = torch.einsum("bsd,df->bsf", x, w_gate)
        u = torch.einsum("bsd,df->bsf", x, w_up)
        h = _gelu(g) * u
        return torch.einsum("bsf,fd->bsd", h, w_down)

    x = A_x
    x = x + _A_ref(_layernorm(x, axis=-1), A_wqkv, A_wo)
    x = x + _B_ref(_layernorm(x, axis=-1), B_w_gate, B_w_up, B_w_down)
    x = x + _A_ref(_layernorm(x, axis=-1), A_wqkv, A_wo)
    x = x + _B_ref(_layernorm(x, axis=-1), B_w_gate, B_w_up, B_w_down)
    return x
```

```cpp
#include <hip/hip_runtime.h>
#include <hip/hip_bf16.h>
typedef __hip_bfloat16 bf16;
#define S_ 2048
#define D_ 640
#define H_ 32
#define DH_ 20
#define W_ 256
#define DF_ 1728

__global__ void k_ln(const float* x, float* y) {
  int row = blockIdx.x; int t = threadIdx.x;
  __shared__ float red[256];
  float s = 0; for (int i = t; i < D_; i += 256) s += x[row * D_ + i];
  red[t] = s; __syncthreads();
  for (int o = 128; o > 0; o >>= 1) { if (t < o) red[t] += red[t + o]; __syncthreads(); }
  float m = red[0] / D_; __syncthreads();
  float v = 0; for (int i = t; i < D_; i += 256) { float d = x[row * D_ + i] - m; v += d * d; }
  red[t] = v; __syncthreads();
  for (int o = 128; o > 0; o >>= 1) { if (t < o) red[t] += red[t + o]; __syncthreads(); }
  float r = rsqrtf(red[0] / D_ + 1e-5f);
  for (int i = t; i < D_; i += 256) y[row * D_ + i] = (x[row * D_ + i] - m) * r;
}
__global__ void k_gemm(const float* A, const bf16* B, float* C, const float* R, int M, int N, int K) {
  int n = blockIdx.x * 64 + (threadIdx.x & 63); int m = blockIdx.y * 4 + (threadIdx.x >> 6);
  if (n >= N) return;
  float acc = 0;
  for (int k = 0; k < K; ++k) acc += A[m * K + k] * __bfloat162float(B[k * N + n]);
  if (R) acc += R[m * N + n];
  C[m * N + n] = acc;
}
__global__ void k_attn(const float* qkv, float* o) {
  int q = blockIdx.x * 64 + threadIdx.x; int h = blockIdx.y;
  float qv[DH_]; for (int d = 0; d < DH_; ++d) qv[d] = qkv[q * 3 * D_ + h * DH_ + d];
  float m = -1e30f; int k0 = q - W_ + 1; if (k0 < 0) k0 = 0;
  float sc = rsqrtf((float)DH_);
  for (int k = k0; k <= q; ++k) { float s = 0; for (int d = 0; d < DH_; ++d) s += qv[d] * qkv[k * 3 * D_ + D_ + h * DH_ + d]; m = fmaxf(m, s * sc); }
  float l = 0; float acc[DH_]; for (int d = 0; d < DH_; ++d) acc[d] = 0;
  for (int k = k0; k <= q; ++k) { float s = 0; for (int d = 0; d < DH_; ++d) s += qv[d] * qkv[k * 3 * D_ + D_ + h * DH_ + d];
    float p = __expf(s * sc - m); l += p; for (int d = 0; d < DH_; ++d) acc[d] += p * qkv[k * 3 * D_ + 2 * D_ + h * DH_ + d]; }
  for (int d = 0; d < DH_; ++d) o[q * D_ + h * DH_ + d] = acc[d] / l;
}
__global__ void k_geglu(const float* g, const float* u, float* h, int n) {
  int i = blockIdx.x * 256 + threadIdx.x; if (i >= n) return;
  float x = g[i]; float t = tanhf(0.7978845608028654f * (x + 0.044715f * x * x * x));
  h[i] = 0.5f * x * (1.f + t) * u[i];
}
__global__ void k_cvt_in(const bf16* a, float* b, int n) { int i = blockIdx.x * 256 + threadIdx.x; if (i < n) b[i] = __bfloat162float(a[i]); }
__global__ void k_cvt_out(const float* a, bf16* b, int n) { int i = blockIdx.x * 256 + threadIdx.x; if (i < n) b[i] = __float2bfloat16(a[i]); }

extern "C" void kernel_launch(void* const* d_in, const int* in_sizes, int n_in, void* d_out, int out_size, void* d_ws, size_t ws_size, hipStream_t stream) {
  const bf16* x = (const bf16*)d_in[0]; const bf16* wqkv = (const bf16*)d_in[1]; const bf16* wo = (const bf16*)d_in[2];
  const bf16* wg = (const bf16*)d_in[4]; const bf16* wu = (const bf16*)d_in[5]; const bf16* wd = (const bf16*)d_in[6];
  float* ws = (float*)d_ws;
  float* X = ws; float* X2 = X + S_ * D_; float* XN = X2 + S_ * D_; float* QKV = XN + S_ * D_; float* O = QKV + S_ * 3 * D_;
  float* G = O + S_ * D_; float* U = G + S_ * DF_; float* Hh = U + S_ * DF_;
  k_cvt_in<<<S_ * D_ / 256, 256, 0, stream>>>(x, X, S_ * D_);
  for (int l = 0; l < 2; ++l) {
    k_ln<<<S_, 256, 0, stream>>>(X, XN);
    k_gemm<<<dim3(30, S_ / 4), 256, 0, stream>>>(XN, wqkv, QKV, nullptr, S_, 3 * D_, D_);
    k_attn<<<dim3(S_ / 64, H_), 64, 0, stream>>>(QKV, O);
    k_gemm<<<dim3(10, S_ / 4), 256, 0, stream>>>(O, wo, X2, X, S_, D_, D_);
    k_ln<<<S_, 256, 0, stream>>>(X2, XN);
    k_gemm<<<dim3(27, S_ / 4), 256, 0, stream>>>(XN, wg, G, nullptr, S_, DF_, D_);
    k_gemm<<<dim3(27, S_ / 4), 256, 0, stream>>>(XN, wu, U, nullptr, S_, DF_, D_);
    k_geglu<<<S_ * DF_ / 256, 256, 0, stream>>>(G, U, Hh, S_ * DF_);
    k_gemm<<<dim3(10, S_ / 4), 256, 0, stream>>>(Hh, wd, X, X2, S_, D_, DF_);
  }
  k_cvt_out<<<S_ * D_ / 256, 256, 0, stream>>>(X, (bf16*)d_out, S_ * D_);
}
```

```cpp
#include <hip/hip_runtime.h>
#include <stdint.h>

typedef unsigned u32;
typedef __attribute__((ext_vector_type(4))) unsigned u32x4;
typedef __attribute__((ext_vector_type(2))) unsigned u32x2;
typedef __attribute__((ext_vector_type(8))) __bf16 bf16x8;
typedef __attribute__((ext_vector_type(2))) __bf16 bf16x2;
typedef __attribute__((ext_vector_type(4))) short s16x4;
typedef __attribute__((ext_vector_type(2))) float f32x2;
typedef __attribute__((ext_vector_type(4))) float f32x4;
typedef __attribute__((ext_vector_type(16))) float f32x16;
#define LAS __attribute__((address_space(3)))
#define GAS __attribute__((address_space(1)))
#define RLX_AGENT __ATOMIC_RELAXED, __HIP_MEMORY_SCOPE_AGENT
#define DEV __device__ __forceinline__
typedef __amdgpu_buffer_rsrc_t rsrc_t;

constexpr u32 KVT = 5248;
constexpr u32 OFF_FLAG = 0;
constexpr u32 OFF_WQ = 1u << 20;
constexpr u32 OFF_WO = OFF_WQ + 16 * 163840;
constexpr u32 OFF_WG = OFF_WO + 8 * 102400;
constexpr u32 OFF_WD = OFF_WG + 16 * 286720;
constexpr u32 OFF_CSQ = OFF_WD + 8 * 286720;
constexpr u32 OFF_CSG = OFF_CSQ + 16 * 4 * 128 * 4;
constexpr u32 OFF_X = OFF_CSG + 16 * 8 * 224 * 4;
constexpr u32 XSZ = 64 * 40960;
constexpr u32 OFF_ST = OFF_X + 4 * XSZ;
constexpr u32 STSZ = 16 * 8 * 128 * 8;
constexpr u32 OFF_KV = OFF_ST + 4 * STSZ;
constexpr u32 KVSZ = 16 * 64 * KVT;
constexpr u32 OFF_O = OFF_KV + 2 * KVSZ;
constexpr u32 OSZ = 128 * 20480;
constexpr u32 OFF_H = OFF_O + 2 * OSZ;
constexpr u32 HSZ = 128 * 57344;
constexpr u32 OFF_END = OFF_H + 2 * HSZ;
constexpr u32 OFF_STAMP = 96u << 20;
enum { F_X = 0, F_KV = 2, F_O = 3, F_X2 = 5, F_H = 7, F_TMO = 31 };

constexpr u32 CH = 20480;
constexpr u32 L_SCR = 6 * CH;
constexpr u32 L_CSQ = L_SCR + 32768;
constexpr u32 L_CSG = L_CSQ + 512;
constexpr u32 L_RSTD = L_CSG + 896;
constexpr u32 L_NMR = L_RSTD + 512;
constexpr u32 L_BNC = L_NMR + 512;
constexpr u32 L_CNT = L_BNC + 2048;
constexpr u32 LDS_BYTES = 163840;

#define BAR() do { __builtin_amdgcn_sched_barrier(0); asm volatile("s_barrier" ::: "memory"); __builtin_amdgcn_sched_barrier(0); } while (0)
#define BAR_L() do { __builtin_amdgcn_sched_barrier(0); asm volatile("s_waitcnt lgkmcnt(0)\n\ts_barrier" ::: "memory"); __builtin_amdgcn_sched_barrier(0); } while (0)
#define WAITV(n) asm volatile("s_waitcnt vmcnt(%0)" :: "i"(n) : "memory")
#define PIN() __builtin_amdgcn_sched_barrier(0)

#define ST(i) do {} while (0)

DEV float asf(u32 x) { return __builtin_bit_cast(float, x); }
DEV u32 pk2(float a, float b) { f32x2 v = {a, b}; bf16x2 r = __builtin_convertvector(v, bf16x2); return __builtin_bit_cast(u32, r); }
DEV float bflo(u32 v) { return __builtin_bit_cast(float, v << 16); }
DEV float bfhi(u32 v) { return __builtin_bit_cast(float, v & 0xffff0000u); }
DEV u32x4 ld16(rsrc_t r, u32 vo, u32 so) { return __builtin_amdgcn_raw_buffer_load_b128(r, vo, so, 16); }
DEV u32x2 ld8(rsrc_t r, u32 vo, u32 so) { return __builtin_amdgcn_raw_buffer_load_b64(r, vo, so, 16); }
DEV u32 ld4(rsrc_t r, u32 vo, u32 so) { return __builtin_amdgcn_raw_buffer_load_b32(r, vo, so, 16); }
DEV void st16(u32x4 v, rsrc_t r, u32 vo, u32 so) { __builtin_amdgcn_raw_buffer_store_b128(v, r, vo, so, 16); }
DEV void st8(u32x2 v, rsrc_t r, u32 vo, u32 so) { __builtin_amdgcn_raw_buffer_store_b64(v, r, vo, so, 16); }
DEV void st4(u32 v, rsrc_t r, u32 vo, u32 so) { __builtin_amdgcn_raw_buffer_store_b32(v, r, vo, so, 16); }
template <typename T> DEV T lds_ld(u32 a) { return *(LAS T*)a; }
template <typename T> DEV void lds_st(u32 a, T v) { *(LAS T*)a = v; }
DEV f32x16 mm32(u32x4 a, u32x4 b, f32x16 c) { return __builtin_amdgcn_mfma_f32_32x32x16_bf16(__builtin_bit_cast(bf16x8, a), __builtin_bit_cast(bf16x8, b), c, 0, 0, 0); }
DEV f32x16 mm32p(u32x4 a, u32x4 b, f32x16 c) { f32x16 r = mm32(a, b, c); asm volatile("" : "+v"(r)); return r; }
DEV f32x16 mm32k8(u32x2 a, u32x2 b, f32x16 c) { return __builtin_amdgcn_mfma_f32_32x32x8bf16_1k(__builtin_bit_cast(s16x4, a), __builtin_bit_cast(s16x4, b), c, 0, 0, 0); }
DEV f32x4 mm16(u32x4 a, u32x4 b, f32x4 c) { return __builtin_amdgcn_mfma_f32_16x16x32_bf16(__builtin_bit_cast(bf16x8, a), __builtin_bit_cast(bf16x8, b), c, 0, 0, 0); }
DEV f32x4 mm16p(u32x4 a, u32x4 b, f32x4 c) { f32x4 r = mm16(a, b, c); asm volatile("" : "+v"(r)); return r; }
DEV float swap32_max(float v) { u32 x = __builtin_bit_cast(u32, v); auto r = __builtin_amdgcn_permlane32_swap(x, x, false, false); return fmaxf(asf(r[0]), asf(r[1])); }
DEV float swap32_add(float v) { u32 x = __builtin_bit_cast(u32, v); auto r = __builtin_amdgcn_permlane32_swap(x, x, false, false); return asf(r[0]) + asf(r[1]); }
DEV float swap16_add(float v) { u32 x = __builtin_bit_cast(u32, v); auto r = __builtin_amdgcn_permlane16_swap(x, x, false, false); return asf(r[0]) + asf(r[1]); }
DEV float dot2(u32 a, u32 b, float c) { return __builtin_amdgcn_fdot2_f32_bf16(__builtin_bit_cast(bf16x2, a), __builtin_bit_cast(bf16x2, b), c, false); }

template <u32 SRC, u32 DST> DEV void dma5(rsrc_t r, u32 vo, u32 so, u32 hl) {
  u32 t;
  asm volatile("s_add_u32 m0, %4, %6\n\ts_add_u32 %0, %3, %5\n\t"
               "buffer_load_dwordx4 %1, %2, %0 offen lds\n\t"
               "buffer_load_dwordx4 %1, %2, %0 offen offset:1024 lds\n\t"
               "buffer_load_dwordx4 %1, %2, %0 offen offset:2048 lds\n\t"
               "buffer_load_dwordx4 %1, %2, %0 offen offset:3072 lds\n\t"
               "s_add_u32 m0, m0, 0x1000\n\ts_add_u32 %0, %0, 0x1000\n\t"
               "buffer_load_dwordx4 %1, %2, %0 offen lds"
               : "=&s"(t) : "v"(vo), "s"(r), "s"(so), "s"(hl), "i"(SRC), "i"(DST) : "memory", "scc");
}
template <typename T> DEV T anyv() { T r; asm volatile("" : "=v"(r)); return r; }
#define OPQV(x) asm volatile("" : "+v"(x))
#define OPQS(x) asm volatile("" : "+s"(x))
#define LANES() int lane; asm volatile("v_mbcnt_lo_u32_b32 %0, -1, 0\n\tv_mbcnt_hi_u32_b32 %0, -1, %0" : "=v"(lane)); const int l31 = lane & 31, hh = lane >> 5, l15 = lane & 15, g = lane >> 4; (void)l31; (void)hh; (void)l15; (void)g; \
  Ctx c; c.rs = rs; c.l16 = lane * 16; c.l16h = lane * 16 + 65536; OPQV(c.l16h); \
  u32 pk_ = pk; OPQS(pk_); const u32 rb = pk_ & 15, s = (pk_ >> 4) & 15, wq = (pk_ >> 8) & 3, layer = pk_ >> 12, cs = s >> 1, half = s & 1; \
  GAS u32* gfl = (GAS u32*)(ws + OFF_FLAG) + (rb * 16384 + layer * 384); GAS u32* fl = gfl + s * 1024; GAS u32* tmo = (GAS u32*)(ws + OFF_FLAG) + F_TMO * 32; \
  const u32 hl = wq * 5120, wq_so = OFF_WQ + s * 163840 + hl, wo_so = OFF_WO + cs * 102400 + hl, wg_so = OFF_WG + s * 286720 + hl, wd_so = OFF_WD + cs * 286720 + hl; \
  const u32 G = rb * 8 + half * 4 + wq, xe_s = OFF_X + ((G >> 1) * 40 + 5 * cs) * 1024 + (G & 1) * 256, kvb = OFF_KV + layer * KVSZ + s * 64 * KVT; \
  (void)fl; (void)tmo; (void)wq_so; (void)wo_so; (void)wg_so; (void)wd_so; (void)xe_s; (void)kvb;

DEV void poll_ge(GAS u32* p, u32 tgt, GAS u32* tmo) {
  u32 spins = 0;
  while (__builtin_amdgcn_readfirstlane(__hip_atomic_load(p, RLX_AGENT)) < tgt) {
    if (++spins > (1u << 16)) {
      if ((spins & 255u) == 0 && (spins > (1u << 20) || __builtin_amdgcn_readfirstlane(__hip_atomic_load(tmo, RLX_AGENT)) != 0)) { __hip_atomic_store(tmo, 1u, RLX_AGENT); break; }
    }
  }
  __builtin_amdgcn_fence(__ATOMIC_ACQUIRE, "wavefront");
  asm volatile("" ::: "memory");
}

DEV void publish16(u32 cnt_lds, u32 nw, GAS u32* fl, int lane) {
  WAITV(0);
  u32 old = 0;
  if (lane == 0) old = __hip_atomic_fetch_add((LAS u32*)cnt_lds, 1u, __ATOMIC_RELAXED, __HIP_MEMORY_SCOPE_WORKGROUP);
  old = __builtin_amdgcn_readfirstlane(old);
  if (old == nw - 1 && lane < 16) __hip_atomic_fetch_add(fl + lane * 1024, 1u, RLX_AGENT);
}

constexpr int PD = 4;
constexpr int ALA = 12;

template <int P0> constexpr u32 frag_lds(int f) { return (u32)(((P0 + f / 20) % 6) * CH + (f % 20) * 1024); }

struct Ctx { rsrc_t rs; u32 l16, l16h; };
DEV u32x4 ring_ld(const Ctx& c, u32 off) { return off < 65536 ? lds_ld<u32x4>(c.l16 + off) : lds_ld<u32x4>(c.l16h + (off - 65536)); }

constexpr int gu_tile(int f) { return f < 90 ? f % 3 : f < 100 ? 0 : f < 110 ? 1 : f < 120 ? 2 : f / 40; }
constexpr int gu_ks(int f) { return f < 90 ? f / 3 : f < 120 ? 30 + (f - 90) % 10 : f % 40; }

template <int F> struct IC { static constexpr int v = F; };
template <int B, int E, typename Fn> DEV void sfor(Fn&& fn) { if constexpr (B < E) { fn(IC<B>{}); sfor<B + 1, E>(fn); } }

template <int P0, int NS, bool BARS>
DEV void gemm_k2(const Ctx& c, u32 kh, u32 vo0, u32 vo1, u32 so, f32x4 (&acc)[2][5]) {
  u32x4 B[NS][2], W[NS * 5];
  u32 lb = c.l16 + kh * 10240, lbh = lb + 65536; OPQV(lb); OPQV(lbh);
  auto wld = [&](auto fi) { constexpr int f = decltype(fi)::v; constexpr int st = f / 5, jn = f % 5;
    constexpr u32 off = (u32)(((P0 + st / 2) % 6) * CH + ((st % 2) * 5 + jn) * 1024);
    W[f] = off < 65536 ? lds_ld<u32x4>(lb + off) : lds_ld<u32x4>(lbh + (off - 65536)); };
  sfor<0, ALA / 2>([&](auto i) { constexpr int s = decltype(i)::v; B[s][0] = ld16(c.rs, vo0 + (s & 3) * 1024, so + (s >> 2) * 4096); B[s][1] = ld16(c.rs, vo1 + (s & 3) * 1024, so + (s >> 2) * 4096); });
  if constexpr (!BARS) BAR();
  sfor<0, PD>([&](auto i) { wld(i); });
  PIN();
  sfor<0, NS * 5>([&](auto fi) {
    constexpr int f = decltype(fi)::v, st = f / 5, jn = f % 5;
    acc[0][jn] = mm16p(W[f], B[st][0], acc[0][jn]);
    if constexpr (f + PD < NS * 5) wld(IC<f + PD>{});
    PIN();
    acc[1][jn] = mm16p(W[f], B[st][1], acc[1][jn]);
    if constexpr (jn < 2 && st + ALA / 2 < NS) { constexpr int s = st + ALA / 2; B[s][jn] = ld16(c.rs, (jn ? vo1 : vo0) + (s & 3) * 1024, so + (s >> 2) * 4096); }
    PIN();
    if constexpr (BARS && f % 10 == 9 && f / 10 < 12) BAR();
  });
}

__global__ void __launch_bounds__(512, 2) mega(const unsigned short* __restrict__ xin, unsigned short* __restrict__ out, unsigned char* __restrict__ wsp) {
  GAS unsigned char* ws = (GAS unsigned char*)wsp;
  const int tid = threadIdx.x, w = __builtin_amdgcn_readfirstlane(tid >> 6);
  const int bx = blockIdx.x & 7, br = blockIdx.x >> 3;
  const bool isC = w < 4;
  u32 pk = ((bx & 3) * 4 + (br & 3)) | (((bx >> 2) * 8 + (br >> 2)) << 4) | ((w & 3) << 8);
  const rsrc_t rs = __builtin_amdgcn_make_buffer_rsrc((void*)wsp, 0, OFF_END, 0x00020000);
#define XE_A ((g >> 1) * 1024 + (l15 + 32 * (g & 1)) * 16)
#define XE_C (4096 + (l15 + 32 * (g >> 1)) * 16 + (g & 1) * 8)
  if (!isC) {
    LANES();
    sfor<0, 6>([&](auto i) { constexpr int k = decltype(i)::v; dma5<(k) * CH, (k) * CH>(rs, c.l16, wq_so, hl); });
    const int ht = tid - 256;
    if (ht < 128) { float v = 0; for (int i = 0; i < 4; ++i) v += __builtin_bit_cast(float, ld4(rs, ht * 4 + i * 512, OFF_CSQ + s * 2048)); lds_st<float>(L_CSQ + ht * 4, v); }
    if (ht < 224) { float v = 0; for (int i = 0; i < 8; ++i) v += __builtin_bit_cast(float, ld4(rs, ht * 4 + i * 896, OFF_CSG + s * 7168)); lds_st<float>(L_CSG + ht * 4, v); }
    if (ht >= 240) lds_st<u32>(L_CNT + (ht - 240) * 4, 0u);
  }

#pragma unroll 1
  for (; pk < 8192; pk += 4096) {
    u32x4 qm; u32 qr;
    ST(0);
    if (isC) {
      LANES();
      if (layer) poll_ge(fl + (F_X + (wq >> 1)) * 32, 8, tmo);
      ST(1);
      const u32 xso = OFF_X + layer * 2 * XSZ + (rb * 4 + wq) * 40960;
      u32x4 A[40], W[160];
      sfor<0, ALA>([&](auto i) { constexpr int k = decltype(i)::v; A[k] = ld16(rs, c.l16 + (k & 3) * 1024, xso + (k >> 2) * 4096); });
      BAR();
      f32x16 acc[3] = {};
      sfor<0, PD>([&](auto i) { constexpr int f = decltype(i)::v; W[f] = ring_ld(c, frag_lds<0>(f)); });
      PIN();
      sfor<0, 120>([&](auto fi) {
        constexpr int f = decltype(fi)::v, t = f % 3, ks = f / 3;
        if constexpr (t == 0) acc[0] = mm32p(A[ks], W[f], acc[0]); else acc[t] = mm32p(W[f], A[ks], acc[t]);
        W[f + PD] = ring_ld(c, frag_lds<0>(f + PD));
        if constexpr (t == 0 && ks + ALA < 40) { constexpr int k = ks + ALA; A[k] = ld16(rs, c.l16 + (k & 3) * 1024, xso + (k >> 2) * 4096); }
        PIN();
        if constexpr (f == 39 || f == 99) BAR();
      });
      ST(2);
      const float rstd = lds_ld<float>(L_RSTD + (wq * 32 + l31) * 4), nmr = lds_ld<float>(L_NMR + (wq * 32 + l31) * 4);
      const u32 kso = kvb + (rb * 4 + wq) * KVT;
      const float qsc = 0.22360679774997896f * 1.4426950408889634f;
      u32 qrem0, qrem1;
      {
        float v[16];
        for (int j = 0; j < 4; ++j) { f32x4 q = lds_ld<f32x4>(L_CSQ + (32 + 8 * j + 4 * hh) * 4); for (int i = 0; i < 4; ++i) v[4 * j + i] = acc[1][4 * j + i] * rstd + q[i] * nmr; }
        u32x4 k0 = {pk2(v[0], v[1]), pk2(v[2], v[3]), pk2(v[4], v[5]), pk2(v[6], v[7])};
        u32x4 k1 = {pk2(v[8], v[9]), pk2(v[10], v[11]), pk2(v[12], v[13]), pk2(v[14], v[15])};
        st16(k0, rs, c.l16, kso); st16(k1, rs, c.l16 + 1024, kso);
        for (int j = 0; j < 3; ++j) { f32x4 q = lds_ld<f32x4>(L_CSQ + (64 + 8 * j + 4 * hh) * 4); for (int i = 0; i < 4; ++i) v[4 * j + i] = acc[2][4 * j + i] * rstd + q[i] * nmr; }
        u32x2 kr = {pk2(v[0], v[1]), pk2(v[2], v[3])};
        st8(kr, rs, 2048 + lane * 8, kso);
        qrem0 = pk2(v[4] * qsc, v[5] * qsc); qrem1 = pk2(v[6] * qsc, v[7] * qsc);
        const u32 bb = L_BNC + wq * 512 + (2 * hh) * 64 + (l31 >> 4) * 32 + ((l31 >> 2) & 1) * 16 + ((l31 & 3) + 4 * ((l31 >> 3) & 1)) * 2;
        const u32 p0 = pk2(v[8], v[9]), p1 = pk2(v[10], v[11]);
        lds_st<unsigned short>(bb, (unsigned short)p0); lds_st<unsigned short>(bb + 64, (unsigned short)(p0 >> 16));
        lds_st<unsigned short>(bb + 256, (unsigned short)p1); lds_st<unsigned short>(bb + 320, (unsigned short)(p1 >> 16));
        const float csn = lds_ld<float>(L_CSQ + l31 * 4);
        for (int j = 0; j < 4; ++j) { f32x4 r4 = lds_ld<f32x4>(L_RSTD + (wq * 32 + 8 * j + 4 * hh) * 4), n4 = lds_ld<f32x4>(L_NMR + (wq * 32 + 8 * j + 4 * hh) * 4);
          for (int i = 0; i < 4; ++i) v[4 * j + i] = acc[0][4 * j + i] * r4[i] + csn * n4[i]; }
        u32x4 v0 = {pk2(v[0], v[1]), pk2(v[2], v[3]), pk2(v[4], v[5]), pk2(v[6], v[7])};
        u32x4 v1 = {pk2(v[8], v[9]), pk2(v[10], v[11]), pk2(v[12], v[13]), pk2(v[14], v[15])};
        const u32 vvo = 2560 + (l31 >> 4) * 1344 + hh * 336 + l15 * 16;
        st16(v0, rs, vvo, kso); st16(v1, rs, vvo + 672, kso);
        asm volatile("" ::: "memory");
        u32x4 vr = lds_ld<u32x4>(L_BNC + wq * 512 + l31 * 16);
        u32 ro = 2560 + (l31 >> 4) * 1344 + ((l31 >> 1) & 1) * 672 + (l31 & 1) * 336 + (16 + ((l31 >> 2) & 3)) * 16;
        if (hh) { vr = u32x4{0x3f803f80u, 0x3f803f80u, 0x3f803f80u, 0x3f803f80u}; ro = 2560 + ((l31 >> 2) & 1) * 1344 + ((l31 >> 1) & 1) * 672 + (l31 & 1) * 336 + 320; }
        if (lane < 40) st16(vr, rs, ro, kso);
      }
      PIN();
      ST(3);
      f32x16 aq = {};
      sfor<120, 160>([&](auto fi) {
        constexpr int f = decltype(fi)::v, ks = f - 120;
        aq = mm32p(W[f], A[ks], aq);
        if constexpr (f + PD < 160) W[f + PD] = ring_ld(c, frag_lds<0>(f + PD));
        PIN();
        if constexpr (f == 148) {
          WAITV(0);
          if (lane < 2 && rb + 1 + lane < 16) __hip_atomic_fetch_add(fl + F_KV * 32 + (lane + 1) * 16 * 1024, 1u, RLX_AGENT);
          PIN();
        }
      });
      BAR();
      ST(4);
      {
        float v[16];
        const float r2 = rstd * qsc, n2 = nmr * qsc;
        for (int j = 0; j < 4; ++j) { f32x4 q = lds_ld<f32x4>(L_CSQ + (96 + 8 * j + 4 * hh) * 4); for (int i = 0; i < 4; ++i) v[4 * j + i] = aq[4 * j + i] * r2 + q[i] * n2; }
        qm = u32x4{pk2(v[0], v[1]), pk2(v[2], v[3]), pk2(v[4], v[5]), pk2(v[6], v[7])};
        u32x4 q1 = {pk2(v[8], v[9]), pk2(v[10], v[11]), pk2(v[12], v[13]), pk2(v[14], v[15])};
        qr = qrem0;
        lds_st<u32x4>(L_SCR + wq * 1024 + c.l16, q1);
        lds_st<u32>(L_SCR + 4096 + wq * 256 + lane * 4, qrem1);
      }
    } else {
      LANES();
      WAITV(0);
      BAR_L();
      {
        const u32 sso = OFF_ST + layer * 2 * STSZ + rb * 8192;
        float sm = 0, sq = 0;
        for (int i = 0; i < 4; ++i) { u32x2 p = ld8(rs, (hh * 4 + i) * 1024 + (wq * 32 + l31) * 8, sso); sm += asf(p[0]); sq += asf(p[1]); }
        sm = swap32_add(sm); sq = swap32_add(sq);
        const float mean = sm * (1.f / 640.f), var = fmaxf(sq * (1.f / 640.f) - mean * mean, 0.f);
        const float rstd = __builtin_amdgcn_rsqf(var + 1e-5f);
        if (hh == 0) { lds_st<float>(L_RSTD + (wq * 32 + l31) * 4, rstd); lds_st<float>(L_NMR + (wq * 32 + l31) * 4, -mean * rstd); }
      }
      BAR_L();
      dma5<(6) * CH, (0) * CH>(rs, c.l16, wq_so, hl); dma5<(7) * CH, (1) * CH>(rs, c.l16, wq_so, hl);
      WAITV(0);
      BAR();
      dma5<(0) * CH, (3) * CH>(rs, c.l16, wo_so, hl); dma5<(1) * CH, (4) * CH>(rs, c.l16, wo_so, hl);
      BAR();
      asm volatile("" : "=v"(qm), "=v"(qr));
    }
    {
      LANES();
      const int hd = isC ? 0 : 1, qt = isC ? wq : 3 - wq;
      const int gt = rb * 4 + qt;
      const int nv = gt + 1 < 9 ? gt + 1 : 9;
      const u32 vK = hd * 1024 + c.l16, vR = 2048 + lane * 8 + hd * 4;
      const int vrow = l31 < 18 ? l31 : (l31 == 20 || l31 == 21) ? l31 - 2 : 20;
      const u32 vV = 2560 + hd * 1344 + hh * 336 + vrow * 16;
      const u32 t0 = kvb + gt * KVT;
      u32x4 Km[9], V0[9], V1[9]; u32 Kr[9];
      auto ldt = [&](auto ii) { constexpr int i = decltype(ii)::v; const u32 so = t0 - (i < gt ? i : gt) * KVT;
        Km[i] = ld16(rs, vK, so); Kr[i] = ld4(rs, vR, so); V0[i] = ld16(rs, vV, so); V1[i] = ld16(rs, vV + 672, so); };
      sfor<1, 4>([&](auto ii) { constexpr int i = decltype(ii)::v; Km[i] = anyv<u32x4>(); V0[i] = anyv<u32x4>(); V1[i] = anyv<u32x4>(); Kr[i] = anyv<u32>(); });
      sfor<0, 4>([&](auto ii) { if (decltype(ii)::v <= qt) ldt(ii); });
      if (!isC) { dma5<(2) * CH, (5) * CH>(rs, c.l16, wo_so, hl); dma5<(3) * CH, (0) * CH>(rs, c.l16, wo_so, hl); }
      BAR_L();
      if (!isC) { qm = lds_ld<u32x4>(L_SCR + qt * 1024 + c.l16); qr = lds_ld<u32>(L_SCR + 4096 + qt * 256 + lane * 4); }
      ST(5);
      u32 one2; asm volatile("v_mov_b32 %0, 0x3f803f80" : "=v"(one2));
      f32x16 o = {};
      u32 qpad = 0;
      const f32x16 z16 = {};
      sfor<0, 9>([&](auto ii) {
        constexpr int i = decltype(ii)::v;
        if constexpr (i == 1) {
          if (rb) poll_ge(fl + F_KV * 32, rb >= 2 ? 8 : 4, tmo);
          sfor<1, 9>([&](auto jj) { if (decltype(jj)::v > 3 || decltype(jj)::v > qt) ldt(jj); });
          PIN();
        }
        if (i < nv) {
          f32x16 sc = mm32(Km[i], qm, z16);
          sc = mm32k8(u32x2{Kr[i], one2}, u32x2{qr, qpad}, sc);
          if constexpr (i == 0) {
            float m = -3.0e38f;
            for (int r = 0; r < 16; ++r) { const int key = 8 * (r >> 2) + (r & 3) + 4 * hh; sc[r] = key <= l31 ? sc[r] : -__builtin_inff(); m = fmaxf(m, sc[r]); }
            m = swap32_max(m);
            const u32 ph = pk2(m, 0.f) & 0xffffu; const float mh = bflo(ph);
            const u32 pl = pk2(m - mh, 0.f) & 0xffffu; const float ml = bflo(pl);
            qpad = hh ? 0u : ((ph | (pl << 16)) ^ 0x80008000u);
            const float sh = mh + ml;
            for (int r = 0; r < 16; ++r) sc[r] -= sh;
          }
          if constexpr (i == 8) { int lq = l31 - 4 * hh; OPQV(lq); for (int r = 0; r < 16; ++r) { const int key = 8 * (r >> 2) + (r & 3); sc[r] = key > lq ? sc[r] : -__builtin_inff(); } }
          for (int r = 0; r < 16; ++r) sc[r] = __builtin_amdgcn_exp2f(sc[r]);
          u32x4 p0 = {pk2(sc[0], sc[1]), pk2(sc[2], sc[3]), pk2(sc[4], sc[5]), pk2(sc[6], sc[7])};
          u32x4 p1 = {pk2(sc[8], sc[9]), pk2(sc[10], sc[11]), pk2(sc[12], sc[13]), pk2(sc[14], sc[15])};
          o = mm32(V0[i], p0, o);
          o = mm32(V1[i], p1, o);
        }
      });
      if (__builtin_amdgcn_ballot_w64(!(o[10] < 1.0e12f)) != 0) {
        float m = -3.0e38f;
        for (int pass = 0; pass < 2; ++pass) {
          o = z16;
#pragma unroll 1
          for (int i = 0; i < nv; ++i) {
            const u32 so = t0 - i * KVT;
            u32x4 km = ld16(rs, vK, so), v0 = ld16(rs, vV, so), v1 = ld16(rs, vV + 672, so); u32 kr = ld4(rs, vR, so);
            f32x16 sc = mm32(km, qm, z16);
            sc = mm32k8(u32x2{kr, 0u}, u32x2{qr, 0u}, sc);
            for (int r = 0; r < 16; ++r) { const int key = 8 * (r >> 2) + (r & 3) + 4 * hh; const bool ok = i == 0 ? key <= l31 : (i == 8 ? key > l31 : true); sc[r] = ok ? sc[r] : -__builtin_inff(); }
            if (pass == 0) { for (int r = 0; r < 16; ++r) m = fmaxf(m, sc[r]); }
            else {
              for (int r = 0; r < 16; ++r) sc[r] = __builtin_amdgcn_exp2f(sc[r] - m);
              u32x4 p0 = {pk2(sc[0], sc[1]), pk2(sc[2], sc[3]), pk2(sc[4], sc[5]), pk2(sc[6], sc[7])};
              u32x4 p1 = {pk2(sc[8], sc[9]), pk2(sc[10], sc[11]), pk2(sc[12], sc[13]), pk2(sc[14], sc[15])};
              o = mm32(v0, p0, o); o = mm32(v1, p1, o);
            }
          }
          m = swap32_max(m);
        }
      }
      ST(6);
      const float il = __builtin_amdgcn_rcpf(o[10]);
      u32x4 om = {pk2(o[0] * il, o[1] * il), pk2(o[2] * il, o[3] * il), pk2(o[4] * il, o[5] * il), pk2(o[6] * il, o[7] * il)};
      const u32 orr = pk2(o[8] * il, o[9] * il);
      const u32 oso = OFF_O + layer * OSZ + (rb * 8 + qt * 2) * 20480 + (s >> 3) * 10240;
      const u32 tg = (l31 >> 4) * 20480;
      st16(om, rs, tg + (s & 7) * 1024 + (l15 + 16 * (2 * hd + hh)) * 16, oso);
      st4(orr, rs, tg + (8 + ((s >> 2) & 1)) * 1024 + (l15 + 16 * (s & 3)) * 16 + (2 * hd + hh) * 4, oso);
      publish16(L_CNT + (layer * 4 + 0) * 4, 8, gfl + (F_O + (s >> 3)) * 32, lane);
      ST(7);
    }
    if (isC) {
      u32 res[10];
      auto ldres = [&](int which) { LANES(); const u32 xs = xe_s + (layer * 2 + which) * XSZ;
        u32x4 a = ld16(rs, XE_A, xs), b = ld16(rs, XE_A + 2048, xs); u32x2 d = ld8(rs, XE_C, xs);
        res[0] = a[0]; res[1] = a[1]; res[2] = a[2]; res[3] = a[3]; res[4] = b[0]; res[5] = b[1]; res[6] = b[2]; res[7] = b[3]; res[8] = d[0]; res[9] = d[1]; };
      auto epi = [&](f32x4 (&acc)[2][5], int which) {
        LANES();
        for (int j = 0; j < 5; ++j) lds_st<f32x4>(L_SCR + (wq ^ 1) * 5120 + j * 1024 + c.l16, acc[1][j]);
        BAR_L();
        float v[20];
        for (int j = 0; j < 5; ++j) { f32x4 p = lds_ld<f32x4>(L_SCR + wq * 5120 + j * 1024 + c.l16);
          v[4 * j + 0] = acc[0][j][0] + p[0] + bflo(res[2 * j]); v[4 * j + 1] = acc[0][j][1] + p[1] + bfhi(res[2 * j]);
          v[4 * j + 2] = acc[0][j][2] + p[2] + bflo(res[2 * j + 1]); v[4 * j + 3] = acc[0][j][3] + p[3] + bfhi(res[2 * j + 1]); }
        for (int j = 0; j < 10; ++j) res[j] = pk2(v[2 * j], v[2 * j + 1]);
        if (which == 1 && layer == 1) {
          GAS unsigned char* o = (GAS unsigned char*)out + (size_t)(G * 16 + l15) * 1280 + cs * 160;
          *(GAS u32x4*)(o + g * 16) = u32x4{res[0], res[1], res[2], res[3]};
          *(GAS u32x4*)(o + 64 + g * 16) = u32x4{res[4], res[5], res[6], res[7]};
          *(GAS u32x2*)(o + 128 + g * 8) = u32x2{res[8], res[9]};
        } else {
          const int xi = layer * 2 + 1 + which;
          st16(u32x4{res[0], res[1], res[2], res[3]}, rs, XE_A, xe_s + xi * XSZ);
          st16(u32x4{res[4], res[5], res[6], res[7]}, rs, XE_A + 2048, xe_s + xi * XSZ);
          st8(u32x2{res[8], res[9]}, rs, XE_C, xe_s + xi * XSZ);
          float sm = 0, sq = 0;
          for (int j = 0; j < 10; ++j) { sm = dot2(res[j], 0x3f803f80u, sm); sq = dot2(res[j], res[j], sq); }
          sm = swap32_add(swap16_add(sm)); sq = swap32_add(swap16_add(sq));
          if (lane < 16) st8(u32x2{__builtin_bit_cast(u32, sm), __builtin_bit_cast(u32, sq)}, rs, cs * 1024 + (half * 64 + wq * 16 + lane) * 8, OFF_ST + xi * STSZ + rb * 8192);
          publish16(L_CNT + (layer * 4 + 1 + 2 * which) * 4, 4, gfl + ((which ? 12 + F_X : F_X2) + half) * 32, lane);
        }
      };
      {
        LANES();
        const u32 kh = wq & 1, th = wq >> 1;
        ldres(0);
        poll_ge(fl + (F_O + kh) * 32, 8, tmo);
        ST(8);
        f32x4 acc[2][5] = {};
        const u32 so = OFF_O + layer * OSZ + (rb * 8 + half * 4 + th * 2) * 20480 + kh * 10240;
        gemm_k2<9, 10, false>(c, kh, c.l16 + kh * 20480, c.l16 + (kh ^ 1) * 20480, so, acc);
        ST(9);
        epi(acc, 0);
        ST(10);
      }
      {
        LANES();
        poll_ge(fl + (F_X2 + (wq >> 1)) * 32, 8, tmo);
        ST(11);
        const u32 xso = OFF_X + (layer * 2 + 1) * XSZ + (rb * 4 + wq) * 40960;
        u32x4 A[40], W[280];
        sfor<0, ALA>([&](auto i) { constexpr int k = decltype(i)::v; A[k] = ld16(rs, c.l16 + (k & 3) * 1024, xso + (k >> 2) * 4096); });
        BAR();
        f32x16 acc[7];
        const f32x16 z16 = {};
        sfor<0, PD>([&](auto i) { constexpr int f = decltype(i)::v; W[f] = ring_ld(c, frag_lds<14>(f)); });
        PIN();
        auto hand = [&](const f32x16& a, int buf) { const u32 b = L_SCR + buf * 16384 + wq * 4096 + c.l16;
          for (int j = 0; j < 4; ++j) lds_st<f32x4>(b + j * 1024, f32x4{a[4 * j], a[4 * j + 1], a[4 * j + 2], a[4 * j + 3]}); };
        sfor<0, 280>([&](auto fi) {
          constexpr int f = decltype(fi)::v, t = gu_tile(f), ks = gu_ks(f);
          if constexpr (ks == 0) acc[t] = mm32p(W[f], A[ks], z16); else acc[t] = mm32p(W[f], A[ks], acc[t]);
          if constexpr (f + PD < 280) W[f + PD] = ring_ld(c, frag_lds<14>(f + PD));
          if constexpr (f < 90 && t == 0 && ks + ALA < 40) { constexpr int k = ks + ALA; A[k] = ld16(rs, c.l16 + (k & 3) * 1024, xso + (k >> 2) * 4096); }
          PIN();
          if constexpr (f == 103) hand(acc[0], 0);
          if constexpr (f == 123) hand(acc[1], 1);
          if constexpr (f == 143) hand(acc[2], 0);
          if constexpr (f == 163) hand(acc[3], 1);
          if constexpr (f == 203) hand(acc[4], 0);
          if constexpr (f == 243) hand(acc[5], 1);
          if constexpr (f == 279) { hand(acc[6], 0); BAR_L(); }
          else if constexpr (f % 20 == 19 && f >= 59) BAR();
        });
        ST(12);
      }
      {
        LANES();
        const u32 kh = wq & 1, th = wq >> 1;
        ldres(1);
        poll_ge(fl + (F_H + kh) * 32, 8, tmo);
        ST(13);
        f32x4 acc[2][5] = {};
        const u32 so = OFF_H + layer * HSZ + (rb * 8 + half * 4 + th * 2) * 57344 + kh * 28672;
        gemm_k2<28, 28, true>(c, kh, c.l16 + kh * 57344, c.l16 + (kh ^ 1) * 57344, so, acc);
        ST(14);
        epi(acc, 1);
        ST(15);
      }
    } else {
      LANES();
      dma5<(4) * CH, (1) * CH>(rs, c.l16, wo_so, hl); dma5<(0) * CH, (2) * CH>(rs, c.l16, wg_so, hl);
      WAITV(0);
      BAR();
      BAR();
      sfor<1, 6>([&](auto i) { constexpr int k = decltype(i)::v; dma5<(k) * CH, (((14 + k) % 6)) * CH>(rs, c.l16, wg_so, hl); WAITV(5); });
      WAITV(0);
      BAR();
      float mean, k1, k2, vv;
      {
        const u32 sso = OFF_ST + (layer * 2 + 1) * STSZ + rb * 8192;
        float sm = 0, sq = 0;
        for (int i = 0; i < 4; ++i) { u32x2 p = ld8(rs, (hh * 4 + i) * 1024 + (wq * 32 + l31) * 8, sso); sm += asf(p[0]); sq += asf(p[1]); }
        sm = swap32_add(sm); sq = swap32_add(sq);
        mean = sm * (1.f / 640.f);
        vv = fmaxf(sq * (1.f / 640.f) - mean * mean, 0.f) + 1e-5f;
        const float rstd = __builtin_amdgcn_rsqf(vv);
        k1 = -2.f * 0.7978845608028654f * 1.4426950408889634f * rstd;
        k2 = k1 * 0.044715f * rstd * rstd;
        mean = -mean;
      }
      const u32 hso = OFF_H + layer * HSZ + (rb * 8 + wq * 2) * 57344 + (s >> 3) * 28672 + ((s >> 1) & 3) * 1024;
      const u32 hvo = (l31 >> 4) * 57344 + (l15 + 16 * (2 * (s & 1) + hh)) * 16;
      auto geglu = [&](int t, int buf) {
        const u32 b = L_SCR + buf * 16384 + wq * 4096 + c.l16;
        f32x4 a[4], q[4];
        for (int j = 0; j < 4; ++j) { a[j] = lds_ld<f32x4>(b + j * 1024); q[j] = lds_ld<f32x4>(L_CSG + (t * 32 + 8 * j + 4 * hh) * 4); }
        float h[8];
        for (int j = 0; j < 8; ++j) {
          const float ag = q[j >> 2][j & 3] * mean + a[j >> 2][j & 3];
          const float au = q[2 + (j >> 2)][j & 3] * mean + a[2 + (j >> 2)][j & 3];
          const float e = __builtin_amdgcn_exp2f(ag * (k2 * (ag * ag) + k1));
          h[j] = (ag * au) * __builtin_amdgcn_rcpf(e * vv + vv);
        }
        st16(u32x4{pk2(h[0], h[1]), pk2(h[2], h[3]), pk2(h[4], h[5]), pk2(h[6], h[7])}, rs, hvo, hso + t * 4096);
      };
      auto refill = [&](auto pi) { constexpr int p = decltype(pi)::v;
        if constexpr (p < 28) dma5<((p - 14)) * CH, ((p % 6)) * CH>(rs, c.l16, wg_so, hl); else dma5<((p - 28)) * CH, ((p % 6)) * CH>(rs, c.l16, wd_so, hl); };
      BAR();
      refill(IC<20>{}); refill(IC<21>{}); refill(IC<22>{});
      sfor<17, 28>([&](auto ci) {
        constexpr int cc = decltype(ci)::v;
        if constexpr (cc >= 18) WAITV(15);
        BAR_L();
        refill(IC<cc + 6>{});
        if constexpr (cc == 19) geglu(0, 0);
        if constexpr (cc == 20) geglu(1, 1);
        if constexpr (cc == 21) geglu(2, 0);
        if constexpr (cc == 22) geglu(3, 1);
        if constexpr (cc == 24) geglu(4, 0);
        if constexpr (cc == 26) geglu(5, 1);
        if constexpr (cc == 27) geglu(6, 0);
      });
      publish16(L_CNT + (layer * 4 + 2) * 4, 4, gfl + (F_H + (s >> 3)) * 32, lane);
      sfor<28, 40>([&](auto ci) {
        constexpr int cc = decltype(ci)::v;
        WAITV(cc <= 36 ? 15 : (39 - cc) * 5);
        BAR();
        if constexpr (cc + 6 < 42) refill(IC<cc + 6>{});
      });
      BAR();
      if (layer == 0) sfor<0, 6>([&](auto i) { constexpr int k = decltype(i)::v; dma5<(k) * CH, (k) * CH>(rs, c.l16, wq_so, hl); WAITV(5); });
    }
  }
}

__global__ void __launch_bounds__(256) prep(const unsigned short* __restrict__ x, const unsigned short* __restrict__ wqkv, const unsigned short* __restrict__ wo,
                                            const unsigned short* __restrict__ wg, const unsigned short* __restrict__ wu, const unsigned short* __restrict__ wd,
                                            unsigned char* __restrict__ ws) {
  __shared__ __attribute__((aligned(16))) unsigned short sm[26624];
  const int u = blockIdx.x, tid = threadIdx.x, lane = tid & 63, wv = tid >> 6;
  if (u < 32) ((u32*)(ws + OFF_FLAG))[(u * 256 + tid) * 32] = 0u;
  if (u < 64) {
    const int s = u >> 2, kb = u & 3;
    for (int i = tid; i < 160 * 30; i += 256) { const int r = i / 30, q = i % 30, p = q / 10, e = q % 10;
      const u32x2 v = __builtin_nontemporal_load((const u32x2*)(wqkv + (size_t)(kb * 160 + r) * 1920 + p * 640 + s * 40 + e * 4));
      *(u32*)(sm + r * 122 + p * 40 + e * 4) = v[0]; *(u32*)(sm + r * 122 + p * 40 + e * 4 + 2) = v[1]; }
    __syncthreads();
    const int rho = lane & 31, hq = lane >> 5;
    int col;
    { const int r16 = rho & 15, dm = 8 * ((r16 >> 2) & 1) + 4 * (r16 >> 3) + (r16 & 3), hd = rho >> 4;
      if (wv == 0) col = 80 + hd * 20 + dm; else if (wv == 1) col = 40 + hd * 20 + dm; else if (wv == 3) col = hd * 20 + dm;
      else { const int reg = 4 * (rho >> 3) + (rho & 3), h2 = (rho >> 2) & 1, grp = reg >> 2, hd2 = (reg >> 1) & 1, d = 16 + 2 * h2 + (reg & 1);
        col = grp == 0 ? 40 + hd2 * 20 + d : grp == 1 ? hd2 * 20 + d : grp == 2 ? 80 + hd2 * 20 + d : -1; } }
    float csum = 0;
    for (int ks = 0; ks < 10; ++ks) {
      u32 o[4];
      for (int j = 0; j < 4; ++j) { const int r = ks * 16 + hq * 8 + 2 * j; const u32 a = col < 0 ? 0u : sm[r * 122 + col], b = col < 0 ? 0u : sm[(r + 1) * 122 + col]; o[j] = a | (b << 16); csum += bflo(a) + bflo(b); }
      const int kg = kb * 10 + ks, f = wv < 3 ? kg * 3 + wv : 120 + kg;
      *(u32x4*)(ws + OFF_WQ + s * 163840 + f * 1024 + lane * 16) = u32x4{o[0], o[1], o[2], o[3]};
    }
    csum = swap32_add(csum);
    if (hq == 0) ((float*)(ws + OFF_CSQ))[(s * 4 + kb) * 128 + wv * 32 + rho] = csum;
  } else if (u < 192) {
    const int s = (u - 64) >> 3, kb = (u - 64) & 7;
    for (int i = tid; i < 80 * 54; i += 256) { const int r = i / 54, q = i % 54, p = q / 27, e = q % 27;
      const u32x2 v = __builtin_nontemporal_load((const u32x2*)((p ? wu : wg) + (size_t)(kb * 80 + r) * 1728 + s * 108 + e * 4));
      *(u32*)(sm + r * 218 + p * 108 + e * 4) = v[0]; *(u32*)(sm + r * 218 + p * 108 + e * 4 + 2) = v[1]; }
    __syncthreads();
    const int rho = lane & 31, hq = lane >> 5, r16 = rho & 15, uu = 8 * ((r16 >> 2) & 1) + 4 * (r16 >> 3) + (r16 & 3);
    for (int t = wv; t < 7; t += 4) {
      const int cc = t * 16 + uu, col = cc < 108 ? (rho >> 4) * 108 + cc : -1;
      float csum = 0;
      for (int ks = 0; ks < 5; ++ks) {
        u32 o[4];
        for (int j = 0; j < 4; ++j) { const int r = ks * 16 + hq * 8 + 2 * j; const u32 a = col < 0 ? 0u : sm[r * 218 + col], b = col < 0 ? 0u : sm[(r + 1) * 218 + col]; o[j] = a | (b << 16); csum += bflo(a) + bflo(b); }
        const int kg = kb * 5 + ks, f = t >= 3 ? t * 40 + kg : kg < 30 ? kg * 3 + t : 90 + t * 10 + (kg - 30);
        *(u32x4*)(ws + OFF_WG + s * 286720 + f * 1024 + lane * 16) = u32x4{o[0], o[1], o[2], o[3]};
      }
      csum = swap32_add(csum);
      if (hq == 0) ((float*)(ws + OFF_CSG))[(s * 8 + kb) * 224 + t * 32 + rho] = csum;
    }
  } else if (u < 264) {
    const bool isO = u < 208;
    const int v = isO ? u - 192 : u - 208;
    const int cs = isO ? v >> 1 : v / 7, part = isO ? v & 1 : v % 7;
    const int nr = isO ? 320 : 256;
    for (int i = tid; i < nr * 10; i += 256) { const int r = i / 10, e = i % 10;
      int row; bool ok = true;
      if (isO) row = part * 320 + r; else { const int cc = part * 16 + (r & 15); row = (r >> 4) * 108 + cc; ok = cc < 108; }
      u32x4 t = {0, 0, 0, 0};
      if (ok) t = __builtin_nontemporal_load((const u32x4*)((isO ? wo : wd) + (size_t)row * 640 + cs * 80 + e * 8));
      for (int j = 0; j < 4; ++j) *(u32*)(sm + r * 82 + e * 8 + 2 * j) = t[j]; }
    __syncthreads();
    const int i16 = lane & 15, kq = lane >> 4, gg = i16 >> 2, rr = i16 & 3;
    const int nfr = isO ? 50 : 40;
    for (int q = wv; q < nfr; q += 4) {
      const int st = q / 5, jn = q % 5;
      const int col = jn < 4 ? (jn >> 1) * 32 + 8 * gg + 4 * (jn & 1) + rr : 64 + 4 * gg + rr;
      u32 o[4];
      for (int j = 0; j < 4; ++j) {
        int r0;
        if (isO) { if (st < 8) r0 = st * 40 + (kq >> 1) * 20 + (kq & 1) * 8 + 2 * j; else r0 = ((st - 8) * 4 + kq) * 40 + (j >> 1) * 20 + 16 + (j & 1) * 2; }
        else r0 = (2 * st + (kq >> 1)) * 16 + (kq & 1) * 8 + 2 * j;
        o[j] = (u32)sm[r0 * 82 + col] | ((u32)sm[(r0 + 1) * 82 + col] << 16);
      }
      int kh, step;
      if (isO) { kh = part; step = st; } else { kh = st >> 2; step = part * 4 + (st & 3); }
      const int f = (step >> 1) * 20 + kh * 10 + (step & 1) * 5 + jn;
      *(u32x4*)(ws + (isO ? OFF_WO + cs * 102400 : OFF_WD + cs * 286720) + f * 1024 + lane * 16) = u32x4{o[0], o[1], o[2], o[3]};
    }
  } else {
    const int T = u - 264;
    const int t = lane & 31, hq = lane >> 5;
    float smv[2] = {0, 0}, sqv[2] = {0, 0};
    for (int k = 0; k < 10; ++k) { const int ks = wv * 10 + k;
      const u32x4 v = *(const u32x4*)(x + (size_t)(T * 32 + t) * 640 + ks * 16 + hq * 8);
      *(u32x4*)(ws + OFF_X + (T * 40 + ks) * 1024 + lane * 16) = v;
      for (int j = 0; j < 4; ++j) { smv[k / 5] = dot2(v[j], 0x3f803f80u, smv[k / 5]); sqv[k / 5] = dot2(v[j], v[j], sqv[k / 5]); } }
    for (int i = 0; i < 2; ++i) { const float a = swap32_add(smv[i]), b = swap32_add(sqv[i]);
      if (hq == 0) *(f32x2*)(ws + OFF_ST + (T >> 2) * 8192 + (wv * 2 + i) * 1024 + ((T & 3) * 32 + t) * 8) = f32x2{a, b}; }
  }
}

extern "C" void kernel_launch(void* const* d_in, const int* in_sizes, int n_in, void* d_out, int out_size, void* d_ws, size_t ws_size, hipStream_t stream) {
  (void)in_sizes; (void)n_in; (void)out_size; (void)ws_size;
  const unsigned short* x = (const unsigned short*)d_in[0];
  (void)hipFuncSetAttribute((const void*)mega, hipFuncAttributeMaxDynamicSharedMemorySize, LDS_BYTES);
  prep<<<328, 256, 0, stream>>>(x, (const unsigned short*)d_in[1], (const unsigned short*)d_in[2], (const unsigned short*)d_in[4], (const unsigned short*)d_in[5], (const unsigned short*)d_in[6], (unsigned char*)d_ws);
  mega<<<256, 512, LDS_BYTES, stream>>>(x, (unsigned short*)d_out, (unsigned char*)d_ws);
}
```

```cpp
#include <hip/hip_runtime.h>
#include <stdint.h>
#if defined(__HIP_DEVICE_COMPILE__)
#pragma clang attribute push(__attribute__((target("no-packed-fp32-ops"))), apply_to = function)
#endif

typedef unsigned u32;
typedef __attribute__((ext_vector_type(4))) unsigned u32x4;
typedef __attribute__((ext_vector_type(2))) unsigned u32x2;
typedef __attribute__((ext_vector_type(8))) __bf16 bf16x8;
typedef __attribute__((ext_vector_type(2))) __bf16 bf16x2;
typedef __attribute__((ext_vector_type(4))) short s16x4;
typedef __attribute__((ext_vector_type(2))) float f32x2;
typedef __attribute__((ext_vector_type(4))) float f32x4;
typedef __attribute__((ext_vector_type(16))) float f32x16;
#define LAS __attribute__((address_space(3)))
#define GAS __attribute__((address_space(1)))
#define RLX_AGENT __ATOMIC_RELAXED, __HIP_MEMORY_SCOPE_AGENT
#define RLX_WG __ATOMIC_RELAXED, __HIP_MEMORY_SCOPE_WORKGROUP
#define DEV __device__ __forceinline__
typedef __amdgpu_buffer_rsrc_t rsrc_t;

constexpr u32 KVT = 5120;
constexpr u32 XT = 43008;
constexpr u32 HG = 55296;
constexpr u32 OFF_FLAG = 0;
constexpr u32 OFF_WQ = 2u << 20;
constexpr u32 OFF_WO = OFF_WQ + 16 * 163840;
constexpr u32 OFF_WG = OFF_WO + 8 * 102400;
constexpr u32 OFF_WD = OFF_WG + 16 * 286720;
constexpr u32 OFF_CS = OFF_WD + 8 * 276480;
constexpr u32 OFF_ZERO = OFF_CS + 16 * 5 * 1536;
constexpr u32 OFF_DUMP = OFF_ZERO + 5120;
constexpr u32 OFF_X = OFF_DUMP + 65536;
constexpr u32 XSZ = 64 * XT;
constexpr u32 OFF_KV = OFF_X + 4 * XSZ;
constexpr u32 KVSZ = 16 * 64 * KVT;
constexpr u32 OFF_O = OFF_KV + 2 * KVSZ;
constexpr u32 OSZ = 128 * 20480;
constexpr u32 OFF_H = OFF_O + 2 * OSZ;
constexpr u32 HSZ = 128 * HG;
constexpr u32 OFF_END = OFF_H + 2 * HSZ;
constexpr u32 OFF_STAMP = 96u << 20;
enum { F_X = 0, F_KV = 4, F_O = 5, F_X2 = 9, F_H = 13, F_TMO = 63 };

constexpr u32 UN = 10240;
constexpr u32 KVL = 5248;
constexpr u32 L_KV = UN;
constexpr u32 L_QH = L_KV + 12 * KVL;
constexpr u32 L_X1 = 6 * UN, L_X2 = 13 * UN;
constexpr u32 L_FULL = 15 * UN;
constexpr u32 L_FREE = L_FULL + 1024;
constexpr u32 L_CS = L_FREE + 2048;
constexpr u32 L_ATT = L_CS + 3072;
constexpr u32 LDS_BYTES = 163840;
constexpr int LF = 86, LS = 81;
constexpr int T_E0 = 49, T_E1 = 53, T_E2 = 60, T_E3 = 70, T_CS = 7, T_KV = 12, T_X2 = 75;

#define POLL_MAX 0x10000
#define SPIN_MAX 0x100000
#define BAR() do { __builtin_amdgcn_sched_barrier(0); asm volatile("s_waitcnt lgkmcnt(0)\n\ts_barrier" ::: "memory"); __builtin_amdgcn_sched_barrier(0); } while (0)
#define WAITV(n) asm volatile("s_waitcnt vmcnt(%0)" :: "i"(n) : "memory")
#define PIN() __builtin_amdgcn_sched_barrier(0)

#define ST(i) do {} while (0)

DEV float asf(u32 x) { return __builtin_bit_cast(float, x); }
DEV u32 asu(float x) { return __builtin_bit_cast(u32, x); }
DEV u32 pk2(float a, float b) { f32x2 v = {a, b}; bf16x2 r = __builtin_convertvector(v, bf16x2); return __builtin_bit_cast(u32, r); }
DEV float bflo(u32 v) { return __builtin_bit_cast(float, v << 16); }
DEV float bfhi(u32 v) { return __builtin_bit_cast(float, v & 0xffff0000u); }
DEV u32x4 ld16(rsrc_t r, u32 vo, u32 so) { return __builtin_amdgcn_raw_buffer_load_b128(r, vo, so, 16); }
DEV u32x2 ld8(rsrc_t r, u32 vo, u32 so) { return __builtin_amdgcn_raw_buffer_load_b64(r, vo, so, 16); }
DEV void st16(u32x4 v, rsrc_t r, u32 vo, u32 so) { __builtin_amdgcn_raw_buffer_store_b128(v, r, vo, so, 16); }
template <typename T> DEV T lds_ld(u32 a) { return *(LAS T*)a; }
template <typename T> DEV void lds_st(u32 a, T v) { *(LAS T*)a = v; }
DEV u32x2 lds_tr(u32 a) { return __builtin_bit_cast(u32x2, __builtin_amdgcn_ds_read_tr16_b64_v4i16((LAS s16x4*)a)); }
DEV int lds_word(u32 a) { return __hip_atomic_load((LAS int*)a, RLX_WG); }
DEV void lds_post(u32 a, int v) { __hip_atomic_store((LAS int*)a, v, RLX_WG); }
DEV f32x16 mm32(u32x4 a, u32x4 b, f32x16 c) { return __builtin_amdgcn_mfma_f32_32x32x16_bf16(__builtin_bit_cast(bf16x8, a), __builtin_bit_cast(bf16x8, b), c, 0, 0, 0); }
DEV f32x16 mm32p(u32x4 a, u32x4 b, f32x16 c) { f32x16 r = mm32(a, b, c); asm volatile("" : "+v"(r)); return r; }
DEV f32x16 mm32k8(u32x2 a, u32x2 b, f32x16 c) { return __builtin_amdgcn_mfma_f32_32x32x8bf16_1k(__builtin_bit_cast(s16x4, a), __builtin_bit_cast(s16x4, b), c, 0, 0, 0); }
DEV f32x16 mm32k8p(u32x2 a, u32x2 b, f32x16 c) { f32x16 r = mm32k8(a, b, c); asm volatile("" : "+v"(r)); return r; }
DEV f32x4 mm16(u32x4 a, u32x4 b, f32x4 c) { return __builtin_amdgcn_mfma_f32_16x16x32_bf16(__builtin_bit_cast(bf16x8, a), __builtin_bit_cast(bf16x8, b), c, 0, 0, 0); }
DEV f32x4 mm16p(u32x4 a, u32x4 b, f32x4 c) { f32x4 r = mm16(a, b, c); asm volatile("" : "+v"(r)); return r; }
DEV float swap32_max(float v) { u32 x = asu(v); auto r = __builtin_amdgcn_permlane32_swap(x, x, false, false); return fmaxf(asf(r[0]), asf(r[1])); }
DEV float swap32_add(float v) { u32 x = asu(v); auto r = __builtin_amdgcn_permlane32_swap(x, x, false, false); return asf(r[0]) + asf(r[1]); }
DEV float swap16_add(float v) { u32 x = asu(v); auto r = __builtin_amdgcn_permlane16_swap(x, x, false, false); return asf(r[0]) + asf(r[1]); }
DEV float dot2(u32 a, u32 b, float c) { return __builtin_amdgcn_fdot2_f32_bf16(__builtin_bit_cast(bf16x2, a), __builtin_bit_cast(bf16x2, b), c, false); }
DEV u32 split2(float m) { const u32 ph = pk2(m, 0.f) & 0xffffu; return ph | (pk2(m - bflo(ph), 0.f) << 16); }
DEV u32 mulpk(float a, float b, float r) { u32 p, t; asm volatile("v_mul_f32 %0, %2, %4\n\tv_mul_f32 %1, %3, %4\n\tv_cvt_pk_bf16_f32 %0, %0, %1" : "=&v"(p), "=&v"(t) : "v"(a), "v"(b), "v"(r)); return p; }
DEV float geg_a(float ag, float k1, float k2) { float e; asm volatile("v_mul_f32 %0, %1, %1\n\tv_fma_f32 %0, %3, %0, %2\n\tv_mul_f32 %0, %1, %0\n\tv_exp_f32 %0, %0" : "=&v"(e) : "v"(ag), "v"(k1), "v"(k2)); return e; }
DEV float geg_b(float e, float ag, float au, float vv) { float h; asm volatile("v_fma_f32 %1, %1, %4, %4\n\tv_rcp_f32 %1, %1\n\tv_mul_f32 %0, %2, %3\n\tv_mul_f32 %0, %0, %1" : "=&v"(h), "+v"(e) : "v"(ag), "v"(au), "v"(vv)); return h; }
DEV u32 geg_c(float e, float ag, float au, float vv, float h0) { u32 h; asm volatile("v_fma_f32 %1, %1, %4, %4\n\tv_rcp_f32 %1, %1\n\tv_mul_f32 %0, %2, %3\n\tv_mul_f32 %0, %0, %1\n\tv_cvt_pk_bf16_f32 %0, %5, %0" : "=&v"(h), "+v"(e) : "v"(ag), "v"(au), "v"(vv), "v"(h0)); return h; }

DEV void dma5(rsrc_t r, u32 vo, u32 so, u32 ld) {
  u32 t;
  asm volatile("s_mov_b32 m0, %4\n\ts_add_u32 %0, %3, 0x1000\n\t"
               "buffer_load_dwordx4 %1, %2, %3 offen lds\n\t"
               "buffer_load_dwordx4 %1, %2, %3 offen offset:1024 lds\n\t"
               "buffer_load_dwordx4 %1, %2, %3 offen offset:2048 lds\n\t"
               "buffer_load_dwordx4 %1, %2, %3 offen offset:3072 lds\n\t"
               "s_add_u32 m0, m0, 0x1000\n\ts_nop 0\n\t"
               "buffer_load_dwordx4 %1, %2, %0 offen lds"
               : "=&s"(t) : "v"(vo), "s"(r), "s"(so), "s"(ld) : "memory", "scc");
}
DEV void dma5kv(rsrc_t r, u32 vo, u32 so, u32 ld) {
  u32 t;
  asm volatile("s_mov_b32 m0, %4\n\ts_add_u32 %0, %3, 0x1000\n\t"
               "buffer_load_dwordx4 %1, %2, %3 offen sc1 lds\n\t"
               "buffer_load_dwordx4 %1, %2, %3 offen offset:1024 sc1 lds\n\t"
               "buffer_load_dwordx4 %1, %2, %3 offen offset:2048 sc1 lds\n\t"
               "buffer_load_dwordx4 %1, %2, %3 offen offset:3072 sc1 lds\n\t"
               "s_add_u32 m0, m0, 0x1080\n\ts_nop 0\n\t"
               "buffer_load_dwordx4 %1, %2, %0 offen sc1 lds"
               : "=&s"(t) : "v"(vo), "s"(r), "s"(so), "s"(ld) : "memory", "scc");
}
template <typename T> DEV T anyv() { T r; asm volatile("" : "=v"(r)); return r; }
template <u32 K> DEV u32 vk() { u32 r; asm volatile("v_mov_b32 %0, %1" : "=v"(r) : "i"(K)); return r; }
#define OPQV(x) asm volatile("" : "+v"(x))
#define OPQS(x) asm volatile("" : "+s"(x))

template <int SLP> DEV void spin_ge(u32 a, int thr) {
  u32 t, n;
  asm volatile("s_mov_b32 %1, %5\n0:\n\tds_read_b32 %0, %2\n\ts_sub_u32 %1, %1, 1\n\ts_waitcnt lgkmcnt(0)\n\ts_cbranch_scc1 1f\n\tv_cmp_gt_i32 vcc, %3, %0\n\ts_cbranch_vccz 1f\n\ts_sleep %4\n\ts_branch 0b\n1:"
               : "=&v"(t), "=&s"(n) : "v"(a), "s"(thr), "i"(SLP), "i"(SPIN_MAX) : "memory", "scc", "vcc");
}
DEV void chk(int v, u32 a, int thr) { if (__builtin_expect(__builtin_amdgcn_ballot_w64(v < thr) != 0, 0)) spin_ge<0>(a, thr); }

DEV void sig(GAS unsigned char* ws, u32 so, u32 vo) { (void)__hip_atomic_fetch_add((GAS u32*)(ws + so + vo), vk<1>(), RLX_AGENT); }
template <int SLP> DEV void poll_ge(GAS unsigned char* ws, u32 so, u32 tgt) {
  GAS u32* p = (GAS u32*)(ws + so + vk<0>());
#pragma unroll 1
  for (int n = POLL_MAX; n > 0; --n) {
    if (__builtin_amdgcn_readfirstlane(__hip_atomic_load(p, RLX_AGENT)) >= tgt) break;
    if (SLP) __builtin_amdgcn_s_sleep(SLP);
  }
  __builtin_amdgcn_fence(__ATOMIC_ACQUIRE, "wavefront");
  asm volatile("" ::: "memory");
}

constexpr int PD = 4;
constexpr int ALA = 12;

template <int F> struct IC { static constexpr int v = F; };
template <int B, int E, typename Fn> DEV void sfor(Fn&& fn) { if constexpr (B < E) { fn(IC<B>{}); sfor<B + 1, E>(fn); } }

struct Ctx { u32 l16, l16h, l16t, mb; };
template <u32 OFF, typename T> DEV T far_ld(const Ctx& c) { return OFF < 65536 ? lds_ld<T>(c.l16 + OFF) : OFF < 131072 ? lds_ld<T>(c.l16h + (OFF - 65536)) : lds_ld<T>(c.l16t + (OFF - 131072)); }
DEV void freed(const Ctx& c) { (void)__builtin_amdgcn_atomic_inc32((LAS u32*)(c.mb + 1024), c.l16h, __ATOMIC_RELAXED, "workgroup"); }

constexpr int gu_tile(int f) { return f < 90 ? f % 3 : f < 120 ? (f - 90) / 10 : f < 200 ? f / 40 : f < 240 ? 5 + (f & 1) : f < 260 ? 5 : 6; }
constexpr int gu_ks(int f) { return f < 90 ? f / 3 : f < 120 ? 30 + f % 10 : f < 200 ? f % 40 : f < 240 ? (f - 200) / 2 : 20 + (f - 240) % 20; }
constexpr int gu_need(int u) { return u < 3 ? 27 + u : u < 15 ? 41 : 27 + u; }
constexpr int a_at(int f) { for (int k = ALA; k < 40; ++k) if ((k - ALA) * 5 / 2 == f) return k; return -1; }
constexpr int geg_t0(int t) { return t == 0 ? 102 : t == 1 ? 119 : t == 2 ? 136 : t == 3 ? 163 : t == 4 ? 203 : t == 5 ? 263 : 282; }
constexpr int hb_time(int b) { return b < 16 ? 217 + b * 3 / 2 : b < 32 ? 257 + (b - 16) * 2 : b < 40 ? 302 + (b - 32) * 2 : 352 + (b - 40) * 3 / 2; }
constexpr int geg_at(int tau) { for (int t = 0; t < 7; ++t) if (tau >= geg_t0(t) && tau <= geg_t0(t) + 16) return t; return -1; }
constexpr int hb_at(int tau) { for (int b = 0; b < 54; ++b) if (hb_time(b) == tau) return b; return -1; }
constexpr int T_RES = 330;
constexpr int vm_after(int t0, int t1) {
  int n = 0;
  for (int t = 0; t < 7; ++t) { const int x = geg_t0(t) + 16; if (x > t0 && x < t1) ++n; }
  for (int b = 0; b < 54; ++b) { const int x = hb_time(b); if (x >= t0 && x < t1) ++n; }
  if (T_RES >= t0 && T_RES < t1) n += 3;
  return n;
}
constexpr int EV_T[4] = {165, 205, 245, 320};
constexpr int ev_at(int tau) { for (int e = 0; e < 4; ++e) if (EV_T[e] == tau) return e; return -1; }
constexpr int EV_TILE[4] = {1, 3, 4, 6};
constexpr int EV_FREE[4] = {30 + 16, 30 + 20, 30 + 24, 58 + 8};

#define LANES() int lane; asm volatile("s_nop 2\n\tv_mbcnt_lo_u32_b32 %0, -1, 0\n\tv_mbcnt_hi_u32_b32 %0, -1, %0" : "=v"(lane)); const int l31 = lane & 31, hh = lane >> 5, l15 = lane & 15, g = lane >> 4; (void)l31; (void)hh; (void)l15; (void)g; \
  u32 pk_ = pk; OPQS(pk_); const u32 rb = pk_ & 15, s = (pk_ >> 4) & 15, wq = (pk_ >> 8) & 3, w = (pk_ >> 8) & 7, hd = w >> 2, layer = pk_ >> 12, cs = s >> 1, half = s & 1; \
  Ctx c; c.l16 = lane * 16; c.l16h = lane * 16 + 65536; c.l16t = lane * 16 + 131072; c.mb = L_FULL + wq * 256 + lane * 4; OPQV(c.l16h); OPQV(c.l16t); OPQV(c.mb); \
  const u32 gfl = OFF_FLAG + rb * 131072 + layer * 2560, fl = gfl + s * 8192; \
  const int fb = layer * LS, eb = layer * LF; \
  const u32 G = rb * 8 + half * 4 + wq, xe_s = OFF_X + (G >> 1) * XT + 5 * cs * 1024 + (G & 1) * 256, kvb = OFF_KV + layer * KVSZ + s * 64 * KVT; \
  (void)fl; (void)hd; (void)xe_s; (void)kvb; (void)fb; (void)eb;

struct Str { rsrc_t rs; u32 l16, fa, pa; int seq, pub; u32 hl; };
template <int N> DEV void stream(Str& t, u32 so, u32 lu, int n, int thr, int dthr) {
  u32 ld = lu * UN + t.hl;
  so += t.hl;
#pragma unroll 1
  for (int i = 0; i < n; ++i) {
    spin_ge<1>(t.fa, thr);
    dma5(t.rs, t.l16, so, ld);
    ++t.seq; so += UN; ld += UN; thr += dthr;
    if (ld >= 15 * UN) ld -= 15 * UN;
    WAITV(N);
    const int p = t.seq - N / 5;
    t.pub = p > t.pub ? p : t.pub;
    lds_post(t.pa, t.pub);
  }
}
DEV void drain(Str& t) { WAITV(0); t.pub = t.seq; lds_post(t.pa, t.pub); }

__global__ void __launch_bounds__(512, 2) mega(unsigned short* __restrict__ out, unsigned char* __restrict__ wsp) {
  GAS unsigned char* ws = (GAS unsigned char*)wsp;
  const int w0 = __builtin_amdgcn_readfirstlane(threadIdx.x >> 6);
  const int bx = blockIdx.x & 7, br = blockIdx.x >> 3;
  const bool isC = w0 < 4;
  u32 pk = ((bx & 3) * 4 + (br & 3)) | (((bx >> 2) * 8 + (br >> 2)) << 4) | (w0 << 8);
  const rsrc_t rs = __builtin_amdgcn_make_buffer_rsrc((void*)wsp, 0, OFF_END, 0x00020000);
#define XE_A ((g >> 1) * 1024 + (l15 + 32 * (g & 1)) * 16)
#define XE_C (4096 + (l15 + 32 * (g >> 1)) * 16)
#define STR_INIT(first) Str t; t.rs = rs; t.l16 = c.l16; t.fa = L_FREE + (lane & 7) * 256; t.pa = L_FULL + g * 256 + (wq * 16 + l15) * 4; t.seq = t.pub = fb + (first); t.hl = wq * 5120;
  {
    LANES();
    lds_post(c.mb + (isC ? 1024 : 2048), isC ? 0 : 16);
    if (!isC) { lds_post(c.mb, g < 2 ? 0 : g == 2 ? T_CS - 1 : T_KV - 1); lds_post(L_ATT + lane * 4, 0); }
    BAR();
    if (w == 4 || w == 5) { STR_INIT(0); stream<15>(t, OFF_WQ + s * 163840, 0, 15, -100, 0); drain(t); }
    if (w >= 6) {
      const u32 so = OFF_CS + s * 7680;
      for (int q = w - 6; q < (w == 6 ? 1 : 3); ++q) {
        u32x4 d;
        for (int j = 0; j < 4; ++j) { float v = 0;
          for (int i = 0; i < 5; ++i) v += asf(__builtin_amdgcn_raw_buffer_load_b32(rs, ((q * 4 + j) * 32 + l31) * 4 + i * 1536, so, 0));
          const u32 p = split2(v), x = hh ? p >> 16 : p & 0xffffu; d[j] = (q * 4 + j < 11) ? x | (x << 16) : 0u; }
        lds_st<u32x4>(L_CS + q * 1024 + c.l16, d);
      }
      if (w == 6) lds_post(L_FULL + g * 256 + (32 + l15) * 4, T_E0 - 1);
    }
  }

#pragma unroll 1
  for (; pk < 8192; pk += 4096) {
    u32x4 qm; u32x2 qr;
    ST(0);
    if (isC) {
      LANES();
      __builtin_amdgcn_s_setprio(2);
      if (layer) poll_ge<0>(ws, fl + (F_X + wq) * 128, 16);
      ST(1);
      const u32 xso = OFF_X + layer * 2 * XSZ + (rb * 4 + wq) * XT;
      u32x4 A[40], W[160];
      sfor<0, ALA>([&](auto i) { constexpr int k = decltype(i)::v; A[k] = ld16(rs, c.l16, xso + k * 1024); });
      u32x2 sp[4];
      for (int i = 0; i < 4; ++i) sp[i] = ld8(rs, hh * 1024 + l31 * 8 + i * 256, xso + 40960);
      spin_ge<0>(c.mb, fb + 1);
      f32x16 acc[3], aq;
      const f32x16 z16 = {};
      sfor<0, PD>([&](auto i) { constexpr int f = decltype(i)::v; W[f] = far_ld<f * 1024, u32x4>(c); });
      PIN();
      float rstd; u32 mo; int fv; u32x4 cq;
      u32 kp[22];
      const u32 kso = kvb + (rb * 4 + wq) * KVT;
      const float qsc = 0.22360679774997896f * 1.4426950408889634f;
      sfor<0, 160>([&](auto fi) {
        constexpr int f = decltype(fi)::v, t = f % 3, ks = f < 120 ? f / 3 : f - 120, n = f + PD;
        if constexpr (f >= 120) aq = mm32p(W[f], A[ks], aq);
        else if constexpr (ks == 0) acc[t] = mm32p(W[f], A[ks], z16); else acc[t] = mm32p(W[f], A[ks], acc[t]);
        if constexpr (n < 160) {
          if constexpr (n % 10 == 0) chk(fv, c.mb, fb + n / 10 + 1);
          W[n] = far_ld<(n % 150) * 1024, u32x4>(c);
          if constexpr (n % 10 == 9) freed(c);
          if constexpr (n % 10 == 5 && n < 150) fv = lds_word(c.mb);
        }
        if constexpr (a_at(f) >= 0) { constexpr int k = a_at(f); A[k] = ld16(rs, c.l16, xso + k * 1024); }
        if constexpr (f == 40) {
          float sm = asf(sp[0][0]) + asf(sp[1][0]) + asf(sp[2][0]) + asf(sp[3][0]), sq = asf(sp[0][1]) + asf(sp[1][1]) + asf(sp[2][1]) + asf(sp[3][1]);
          sm = swap32_add(sm); sq = swap32_add(sq);
          const float mean = sm * (1.f / 640.f);
          rstd = __builtin_amdgcn_rsqf(fmaxf(sq * (1.f / 640.f) - mean * mean, 0.f) + 1e-5f);
          mo = split2(-mean);
          OPQV(rstd); OPQV(mo);
        }
        if constexpr (f == 90) cq = far_ld<L_CS, u32x4>(c);
        if constexpr (f == 98) acc[0] = mm32k8p(u32x2{cq[0], cq[1]}, u32x2{mo, 0u}, acc[0]);
        if constexpr (f == 102) acc[1] = mm32k8p(u32x2{cq[0], cq[1]}, u32x2{0u, mo}, acc[1]);
        if constexpr (f == 106) acc[2] = mm32k8p(u32x2{cq[2], cq[3]}, u32x2{mo, 0u}, acc[2]);
        if constexpr (f == 119) aq = mm32k8p(u32x2{cq[2], cq[3]}, u32x2{0u, mo}, z16);
        if constexpr (f >= 121 && f < 132) {
          constexpr int j = (f - 121) * 2, tt = j / 8, r = (j % 8) * 2;
          if constexpr (j == 18) { const float r2 = rstd * qsc; kp[j] = mulpk(acc[2][4], acc[2][5], r2); kp[j + 1] = mulpk(acc[2][6], acc[2][7], r2); }
          else { kp[j] = mulpk(acc[tt][r], acc[tt][r + 1], rstd); kp[j + 1] = mulpk(acc[tt][r + 2], acc[tt][r + 3], rstd); }
          if constexpr (j < 16 && (j & 2)) {
            constexpr int q = j / 4;
            const u32 vo = q < 2 ? c.l16 : (2 * l31 + hh) * 16;
            const u32x4 v = {kp[j - 2], kp[j - 1], kp[j], kp[j + 1]};
            st16(v, rs, vo, kso + q * 1024);
            lds_st<u32x4>(L_KV + (8 + wq) * KVL + q * 1024 + vo, v);
          }
          if constexpr (j == 20) {
            const u32 vo = (2 * l31 + hh) * 16;
            const u32x4 v = {kp[16], kp[17], kp[20], kp[21]};
            st16(v, rs, vo, kso + 4096);
            lds_st<u32x4>(L_KV + (8 + wq) * KVL + 4224 + vo, v);
            lds_st<u32x2>(L_QH + wq * 1536 + 1024 + lane * 8, u32x2{kp[18], kp[19]});
          }
        }
        PIN();
        if constexpr (f == 152) {
          WAITV(0);
          if (c.l16 < 32 && c.l16 < (15 - rb) * 16) sig(ws, fl + F_KV * 128 + 131072, c.l16 * 8192);
          PIN();
        }
      });
      ST(4);
      {
        const float r2 = rstd * qsc;
        lds_st<u32x4>(L_QH + wq * 1536 + c.l16, u32x4{pk2(aq[8] * r2, aq[9] * r2), pk2(aq[10] * r2, aq[11] * r2), pk2(aq[12] * r2, aq[13] * r2), pk2(aq[14] * r2, aq[15] * r2)});
        qm = u32x4{pk2(aq[0] * r2, aq[1] * r2), pk2(aq[2] * r2, aq[3] * r2), pk2(aq[4] * r2, aq[5] * r2), pk2(aq[6] * r2, aq[7] * r2)};
        qr = u32x2{kp[18], kp[19]};
      }
      __builtin_amdgcn_s_setprio(0);
    } else {
      LANES();
      if (wq < 2) {
        STR_INIT(15);
        stream<0>(t, OFF_WQ + s * 163840 + 15 * UN, 0, 1, eb + 1, 0);
        stream<40>(t, OFF_WO + cs * 102400, 8, 8, eb + 9, 1);
      } else if (wq == 3) {
        spin_ge<2>(L_FREE + (lane & 3) * 256, eb + 8);
        const u32 one = ((int)(7 - rb * 4 - lane) >> 31) & 0x3f803f80u;
        if (lane < 12) lds_st<u32x2>(L_KV + lane * KVL + 4096, u32x2{one, one});
        lds_post(L_FULL + g * 256 + (48 + l15) * 4, fb + T_X2 - 1);
      }
      asm volatile("" : "=v"(qm), "=v"(qr));
    }
    {
      LANES();
      const u32 qt = hd ? 3 - wq : wq;
      f32x16 cm;
      { u32 b = (u32)(l31 - 4 * hh) << 18; OPQV(b);
        for (int r = 0; r < 16; ++r) cm[r] = asf((b - ((8 * (r >> 2) + (r & 3)) << 18)) & 0xff800000u); }
      const u32 tb = L_KV + qt * KVL;
      u32 aK = tb + hd * 1024 + c.l16;
      u32 aR = hh ? tb + 4096 : tb + 4224 + l31 * 32 + hd * 16;
      u32 aV[4];
      { const int q = l15 >> 2, p = l15 & 3;
        for (int r = 0; r < 4; ++r) { const int key = 8 * r + 4 * hh + q;
          aV[r] = !(g & 1) ? tb + 2048 + hd * 1024 + key * 32 + (p & 1) * 16 + (p >> 1) * 8 : p == 0 ? tb + 4224 + key * 32 + hd * 16 + 8 : tb + 4096; OPQV(aV[r]); } }
      OPQV(aK); OPQV(aR); OPQV(cm);
      u32 aA = L_ATT + lane * 4; OPQV(aA);
      BAR();
      if (!isC) { qm = lds_ld<u32x4>(L_QH + qt * 1536 + c.l16); qr = lds_ld<u32x2>(L_QH + qt * 1536 + 1024 + (32 + l31) * 8); }
      ST(5);
      u32x4 Km[9]; u32x2 Kr[9], Vt[9][4]; int av[9];
      auto ldk = [&](auto ii) { constexpr int i = decltype(ii)::v; Km[i] = lds_ld<u32x4>(aK + (8 - i) * KVL); Kr[i] = lds_ld<u32x2>(aR + (8 - i) * KVL); };
      auto ldv = [&](auto ii) { constexpr int i = decltype(ii)::v; for (int r = 0; r < 4; ++r) Vt[i][r] = lds_tr(aV[r] + (8 - i) * KVL); };
      ldk(IC<0>{}); av[1] = lds_word(aA); ldk(IC<1>{}); ldv(IC<0>{});
      const f32x16 z16 = {};
      f32x16 sc[9], o;
      if (hh) qr = u32x2{0u, 0u};
      sc[0] = mm32k8(Kr[0], qr, mm32(Km[0], qm, cm));
      {
        float m = fmaxf(sc[0][0], sc[0][1]);
        for (int r = 2; r < 16; r += 2) m = fmaxf(fmaxf(m, sc[0][r]), sc[0][r + 1]);
        m = swap32_max(m);
        const u32 sh = hh ? split2(-m) : 0u;
        sc[0] = mm32k8p(Kr[0], u32x2{sh, 0u}, sc[0]);
        qr[0] |= sh;
      }
      sfor<0, 9>([&](auto ii) {
        constexpr int i = decltype(ii)::v;
        if constexpr (i >= 1) {
          const int need = i <= (int)qt ? 0 : layer * 2 + (i <= (int)qt + 4 ? 1 : 2);
          if (__builtin_expect(__builtin_amdgcn_ballot_w64(av[i] < need) != 0, 0)) {
            spin_ge<1>(aA, need);
            ldk(ii); ldv(ii);
            if constexpr (i < 8) { av[i + 1] = lds_word(aA); ldk(IC<i + 1>{}); }
            if constexpr (i < 8) sc[i] = mm32k8(Kr[i], qr, mm32(Km[i], qm, z16)); else sc[i] = mm32k8(Kr[i], qr, mm32(Km[i], qm, cm));
            __builtin_amdgcn_s_waitcnt(0xC07F);
          }
        }
        if constexpr (i < 7) sc[i + 1] = mm32k8p(Kr[i + 1], qr, mm32(Km[i + 1], qm, z16));
        if constexpr (i == 7) sc[8] = mm32k8p(Kr[8], qr, mm32(Km[8], qm, cm));
        if constexpr (i < 8) ldv(IC<i + 1>{});
        if constexpr (i < 7) { av[i + 2] = lds_word(aA); ldk(IC<i + 2>{}); }
        PIN();
        float e[16];
        for (int r = 0; r < 8; ++r) e[r] = __builtin_amdgcn_exp2f(sc[i][r]);
        const u32x4 p0 = {pk2(e[0], e[1]), pk2(e[2], e[3]), pk2(e[4], e[5]), pk2(e[6], e[7])};
        const u32x4 v0 = {Vt[i][0][0], Vt[i][0][1], Vt[i][1][0], Vt[i][1][1]}, v1 = {Vt[i][2][0], Vt[i][2][1], Vt[i][3][0], Vt[i][3][1]};
        if constexpr (i == 0) o = mm32p(v0, p0, z16); else o = mm32p(v0, p0, o);
        for (int r = 8; r < 16; ++r) e[r] = __builtin_amdgcn_exp2f(sc[i][r]);
        const u32x4 p1 = {pk2(e[8], e[9]), pk2(e[10], e[11]), pk2(e[12], e[13]), pk2(e[14], e[15])};
        o = mm32p(v1, p1, o);
        PIN();
        if constexpr (i == 0) {
          for (int r = 0; r < 16; ++r) cm[r] = asf(asu(cm[r]) ^ 0xff800000u);
          OPQV(cm);
          if (w >= 6) {
            poll_ge<0>(ws, fl + F_KV * 128, rb >= 2 ? 8 : rb * 4);
            const u32 j = w == 6 ? 6 : 4, src = kvb + (rb * 4 - 8 + j) * KVT;
            dma5kv(rs, c.l16, rb >= 1 ? src + KVT : OFF_ZERO, L_KV + (j + 1) * KVL);
            dma5kv(rs, c.l16, rb >= 1 ? src : OFF_ZERO, L_KV + j * KVL);
            dma5kv(rs, c.l16, rb >= 2 ? src - 3 * KVT : OFF_ZERO, L_KV + (j - 3) * KVL);
            dma5kv(rs, c.l16, rb >= 2 ? src - 4 * KVT : OFF_ZERO, L_KV + (j - 4) * KVL);
            const u32 pa = (aA & ~128u) + (w & 1) * 128;
            WAITV(10); lds_post(pa, layer * 2 + 1);
            WAITV(0); lds_post(pa, layer * 2 + 2);
          }
          PIN();
        }
      });
      if (__builtin_amdgcn_ballot_w64(!(o[12] < 1.0e12f && o[12] > 1.0e-12f)) != 0) {
        const int gt = rb * 4 + qt, nv = gt + 1 < 9 ? gt + 1 : 9;
        if (hh) qr[0] = 0u;
        float m = -3.0e38f;
        for (int pass = 0; pass < 2; ++pass) {
          o = z16;
#pragma unroll 1
          for (int i = 0; i < nv; ++i) {
            const u32 d = (8 - i) * KVL;
            const u32 ma = (i & 7) ? 0u : ~0u, mb = i ? 0u : 0xff800000u;
            f32x16 x;
            for (int r = 0; r < 16; ++r) x[r] = asf((asu(cm[r]) & ma) ^ mb);
            x = mm32k8(lds_ld<u32x2>(aR + d), qr, mm32(lds_ld<u32x4>(aK + d), qm, x));
            if (pass == 0) { for (int r = 0; r < 16; ++r) m = fmaxf(m, x[r]); }
            else {
              for (int r = 0; r < 16; ++r) x[r] = __builtin_amdgcn_exp2f(x[r] - m);
              const u32x4 p0 = {pk2(x[0], x[1]), pk2(x[2], x[3]), pk2(x[4], x[5]), pk2(x[6], x[7])};
              const u32x4 p1 = {pk2(x[8], x[9]), pk2(x[10], x[11]), pk2(x[12], x[13]), pk2(x[14], x[15])};
              const u32x2 a = lds_tr(aV[0] + d), b = lds_tr(aV[1] + d), e = lds_tr(aV[2] + d), f = lds_tr(aV[3] + d);
              o = mm32(u32x4{a[0], a[1], b[0], b[1]}, p0, o); o = mm32(u32x4{e[0], e[1], f[0], f[1]}, p1, o);
            }
          }
          m = swap32_max(m);
        }
      }
      (void)__hip_atomic_fetch_add((LAS u32*)(c.mb + 1024 + hd * 1024), 1 + hd * (LF - 1), RLX_WG);
      ST(6);
      const float il = __builtin_amdgcn_rcpf(o[12]);
      const u32x4 om = {pk2(o[0] * il, o[1] * il), pk2(o[2] * il, o[3] * il), pk2(o[4] * il, o[5] * il), pk2(o[6] * il, o[7] * il)};
      const u32 r0 = pk2(o[8] * il, o[9] * il), r1 = pk2(o[10] * il, o[11] * il);
      const u32x4 orr = {r0, r1, (u32)__builtin_amdgcn_mov_dpp(r0, 0xf5, 0xf, 0xf, true), (u32)__builtin_amdgcn_mov_dpp(r1, 0xf5, 0xf, 0xf, true)};
      const u32 oso = OFF_O + layer * OSZ + (rb * 8 + qt * 2) * 20480 + (s >> 3) * 10240;
      const u32 tg = (l31 >> 4) * 20480;
      st16(om, rs, tg + (s & 7) * 1024 + (l15 + 16 * (2 * hd + hh)) * 16, oso);
      if (!(lane & 33)) st16(orr, rs, tg + 8192 + ((s & 7) * 2 + hd) * 128 + l15 * 8, oso);
      WAITV(0);
      if (lane < 8) sig(ws, gfl + (qt >> 1) * 8192 + (F_O + (s >> 3) + (qt & 1) * 2) * 128, lane * 16384);
      ST(7);
    }
    if (isC) {
      u32 res[10];
      auto ldres = [&](u32 xs, int l15, int g) {
        u32x4 a = ld16(rs, XE_A, xs), b = ld16(rs, XE_A + 2048, xs); u32x2 d = ld8(rs, XE_C + (g & 1) * 8, xs);
        res[0] = a[0]; res[1] = a[1]; res[2] = a[2]; res[3] = a[3]; res[4] = b[0]; res[5] = b[1]; res[6] = b[2]; res[7] = b[3]; res[8] = d[0]; res[9] = d[1]; };
      auto epi = [&](f32x4 (&acc)[2][5], int which) {
        LANES();
        const u32 xb = (which ? L_X2 : L_X1) + c.l16;
        for (int j = 0; j < 5; ++j) lds_st<f32x4>(xb + (wq ^ 1) * 5120 + j * 1024, acc[1][j]);
        freed(c);
        spin_ge<0>((c.mb ^ 256) + 1024, eb + (which ? 85 : 28));
        float v[20];
        for (int j = 0; j < 5; ++j) { f32x4 p = lds_ld<f32x4>(xb + wq * 5120 + j * 1024);
          v[4 * j + 0] = acc[0][j][0] + bflo(res[2 * j]) + p[0]; v[4 * j + 1] = acc[0][j][1] + bfhi(res[2 * j]) + p[1];
          v[4 * j + 2] = acc[0][j][2] + bflo(res[2 * j + 1]) + p[2]; v[4 * j + 3] = acc[0][j][3] + bfhi(res[2 * j + 1]) + p[3]; }
        freed(c);
        for (int j = 0; j < 10; ++j) res[j] = pk2(v[2 * j], v[2 * j + 1]);
        if (which == 1 && layer == 1) {
          GAS unsigned char* o = (GAS unsigned char*)out + (size_t)(G * 16 + l15) * 1280 + cs * 160;
          *(GAS u32x4*)(o + g * 16) = u32x4{res[0], res[1], res[2], res[3]};
          *(GAS u32x4*)(o + 64 + g * 16) = u32x4{res[4], res[5], res[6], res[7]};
          *(GAS u32x2*)(o + 128 + g * 8) = u32x2{res[8], res[9]};
        } else {
          const u32 xs = xe_s + (layer * 2 + 1 + which) * XSZ;
          st16(u32x4{res[0], res[1], res[2], res[3]}, rs, XE_A, xs);
          st16(u32x4{res[4], res[5], res[6], res[7]}, rs, XE_A + 2048, xs);
          float sm = 0, sq = 0;
          for (int j = 0; j < 10; ++j) { sm = dot2(res[j], 0x3f803f80u, sm); sq = dot2(res[j], res[j], sq); }
          sm = swap32_add(swap16_add(sm)); sq = swap32_add(swap16_add(sq));
          auto a = __builtin_amdgcn_permlane16_swap(res[8], asu(sm), false, false);
          auto b = __builtin_amdgcn_permlane16_swap(res[9], asu(sq), false, false);
          const u32x4 t = {a[0], b[0], (u32)__builtin_amdgcn_update_dpp(a[1], a[1], 0xf5, 0x2, 0xf, false), (u32)__builtin_amdgcn_update_dpp(b[1], b[1], 0xf5, 0x2, 0xf, false)};
          const u32 vo = g == 1 ? 40960 - cs * 4864 - (G & 1) * 128 + l15 * 8 : XE_C;
          if (!(g & 1) || (g == 1 && !(lane & 1))) st16(t, rs, vo, xs);
          WAITV(0);
          if (lane < 16) sig(ws, gfl + ((which ? 20 + F_X : F_X2) + half * 2 + (wq >> 1)) * 128, lane * 8192);
        }
      };
      {
        LANES();
        __builtin_amdgcn_s_setprio(2);
        const u32 kh = wq & 1, th = wq >> 1;
        ldres(xe_s + layer * 2 * XSZ, l15, g);
        Ctx k = c; k.l16 += kh * 5120; k.l16h += kh * 5120; k.l16t += kh * 5120;
        u32x4 B[10][2], W[50];
        auto wld = [&](auto ni) { constexpr int n = decltype(ni)::v; W[n] = far_ld<((8 + n / 5) % 15) * UN + (n % 5) * 1024, u32x4>(k); };
        poll_ge<0>(ws, fl + (F_O + wq) * 128, 16);
        ST(8);
        const u32 so = OFF_O + layer * OSZ + (rb * 8 + half * 4 + th * 2) * 20480 + kh * 10240;
        const u32 se[2] = {so + kh * 20480, so + (kh ^ 1) * 20480};
        u32 vr = g * 256 + l15 * 8; OPQV(vr);
        auto bld = [&](auto bi) { constexpr int b = decltype(bi)::v, st = b / 2, e = b % 2;
          if constexpr (st < 8) B[st][e] = ld16(rs, c.l16, se[e] + st * 1024);
          else { const u32x2 x = ld8(rs, vr, se[e] + st * 1024), y = ld8(rs, vr, se[e] + st * 1024 + 128); B[st][e] = u32x4{x[0], x[1], y[0], y[1]}; } };
        sfor<0, ALA>(bld);
        spin_ge<0>(c.mb, fb + 24);
        sfor<0, PD>(wld);
        PIN();
        f32x4 acc[2][5]; const f32x4 z4 = {};
        int fv;
        sfor<0, 50>([&](auto fi) {
          constexpr int f = decltype(fi)::v, st = f / 5, jn = f % 5, n = f + PD;
          acc[0][jn] = mm16p(W[f], B[st][0], st ? acc[0][jn] : z4);
          if constexpr (n < 50) {
            if constexpr (n == 40) chk(fv, c.mb, fb + 26);
            wld(IC<n>{});
            if constexpr (n % 5 == 4) freed(c);
            if constexpr (n == 35) fv = lds_word(c.mb);
          }
          PIN();
          acc[1][jn] = mm16p(W[f], B[st][1], st ? acc[1][jn] : z4);
          if constexpr (f % 2 == 0 && ALA + f / 2 < 20) bld(IC<ALA + f / 2>{});
          PIN();
        });
        ST(9);
        epi(acc, 0);
        ST(10);
      }
      {
        LANES();
        poll_ge<0>(ws, fl + (F_X2 + wq) * 128, 16);
        ST(11);
        const u32 kh = wq & 1, th = wq >> 1;
        const u32 xso = OFF_X + (layer * 2 + 1) * XSZ + (rb * 4 + wq) * XT;
        u32x4 A[40], W[415], B[27][2];
        sfor<0, ALA>([&](auto i) { constexpr int k = decltype(i)::v; A[k] = ld16(rs, c.l16, xso + k * 1024); });
        u32x2 sp[4];
        for (int i = 0; i < 4; ++i) sp[i] = ld8(rs, hh * 1024 + l31 * 8 + i * 256, xso + 40960);
        spin_ge<0>(c.mb, fb + 29);
        f32x16 acc[7];
        f32x4 ad[2][5];
        const f32x16 z16 = {}; const f32x4 z4 = {};
        sfor<0, PD>([&](auto i) { constexpr int f = decltype(i)::v; W[f] = far_ld<(30 + f) * 1024, u32x4>(c); });
        PIN();
        float k1, k2, vv, ge, gh; u32 mo, hp[4]; int fv; u32x2 cq;
        const u32 hso = OFF_H + layer * HSZ + (rb * 8 + wq * 2) * HG + (s >> 3) * 27648 + ((s >> 1) & 3) * 1024;
        u32 hvo = (l31 >> 4) * HG + (l15 + 16 * (2 * (s & 1) + hh)) * 16;
        OPQV(hvo);
        const u32 bso = OFF_H + layer * HSZ + (rb * 8 + half * 4 + th * 2) * HG + kh * 27648, bs0 = bso + kh * HG, bs1 = bso + (kh ^ 1) * HG;
        Ctx k;
        auto side = [&](auto ti) {
          constexpr int tau = decltype(ti)::v;
          if constexpr (geg_at(tau) >= 0) { constexpr int t = geg_at(tau), d = tau - geg_t0(t), j = (d / 2) & 7;
            if constexpr (d == 16) st16(u32x4{hp[0], hp[1], hp[2], hp[3]}, rs, hvo, t < 6 ? hso + t * 4096 : (s & 7) < 6 ? hso + 24576 : OFF_DUMP);
            else if constexpr (d % 2 == 0) ge = geg_a(acc[t][j], k1, k2);
            else if constexpr (j % 2 == 0) gh = geg_b(ge, acc[t][j], acc[t][8 + j], vv);
            else hp[j / 2] = geg_c(ge, acc[t][j], acc[t][8 + j], vv, gh); }
          if constexpr (hb_at(tau) >= 0) { constexpr int b = hb_at(tau), st = b / 2, e = b % 2;
            B[st][e] = ld16(rs, c.l16, (e ? bs1 : bs0) + st * 1024); }
          if constexpr (tau == T_RES) ldres(xe_s + (layer * 2 + 1) * XSZ, (c.l16 >> 4) & 15, c.l16 >> 8);
        };
        auto rel = [&](auto ti) { constexpr int tau = decltype(ti)::v;
          if constexpr (ev_at(tau) >= 0) WAITV(vm_after(geg_t0(EV_TILE[ev_at(tau)]) + 16, tau));
          freed(c); };
        auto ahead = [&](auto ti) { constexpr int tau = decltype(ti)::v, n = tau + PD;
          if constexpr (n < 280) {
            if constexpr (n % 10 == 0 && n >= 30 && gu_need(n / 10) != gu_need(n / 10 - 1)) chk(fv, c.mb, fb + gu_need(n / 10));
            W[n] = far_ld<((30 + n) % 150) * 1024, u32x4>(c);
            if constexpr (n % 10 == 9) rel(ti);
            if constexpr (n % 10 == 5) fv = lds_word(c.mb);
          } else if constexpr (n < 415) { constexpr int d = n - 280;
            if constexpr (d % 5 == 0) { chk(fv, c.mb, fb + 55 + d / 5); if constexpr (d < 130) fv = lds_word(c.mb); }
            W[n] = far_ld<((1 + d / 5) % 15) * UN + (d % 5) * 1024, u32x4>(k);
            if constexpr (d % 5 == 4) rel(ti);
          } };
        sfor<0, 280>([&](auto fi) {
          constexpr int f = decltype(fi)::v, t = gu_tile(f), ks = gu_ks(f);
          if constexpr (ks == 0 && t >= 3) acc[t] = mm32k8p(cq, (t & 1) ? u32x2{0u, mo} : u32x2{mo, 0u}, z16);
          if constexpr (ks == 0 && t < 3) acc[t] = mm32p(W[f], A[ks], z16); else acc[t] = mm32p(W[f], A[ks], acc[t]);
          if constexpr (f == 270) { k = c; k.l16 += kh * 5120; k.l16h += kh * 5120; k.l16t += kh * 5120; OPQV(k.l16); OPQV(k.l16h); OPQV(k.l16t); }
          ahead(fi);
          if constexpr (a_at(f) >= 0) { constexpr int kk = a_at(f); A[kk] = ld16(rs, c.l16, xso + kk * 1024); }
          if constexpr (f == 40) {
            float sm = asf(sp[0][0]) + asf(sp[1][0]) + asf(sp[2][0]) + asf(sp[3][0]), sq = asf(sp[0][1]) + asf(sp[1][1]) + asf(sp[2][1]) + asf(sp[3][1]);
            sm = swap32_add(sm); sq = swap32_add(sq);
            const float mean = sm * (1.f / 640.f);
            vv = fmaxf(sq * (1.f / 640.f) - mean * mean, 0.f) + 1e-5f;
            const float rstd = __builtin_amdgcn_rsqf(vv);
            k1 = -2.f * 0.7978845608028654f * 1.4426950408889634f * rstd;
            k2 = k1 * 0.044715f * rstd * rstd;
            mo = split2(-mean);
            OPQV(k1); OPQV(k2); OPQV(vv); OPQV(mo);
          }
          if constexpr (f == 70) cq = far_ld<L_CS + 1024, u32x2>(c);
          if constexpr (f == 77) acc[0] = mm32k8p(cq, u32x2{mo, 0u}, acc[0]);
          if constexpr (f == 78) { acc[1] = mm32k8p(cq, u32x2{0u, mo}, acc[1]); cq = far_ld<L_CS + 1024 + 8, u32x2>(c); }
          if constexpr (f == 85) acc[2] = mm32k8p(cq, u32x2{mo, 0u}, acc[2]);
          if constexpr (f == 112) cq = far_ld<L_CS + 1024 + 8, u32x2>(c);
          if constexpr (f == 153 || f == 193) cq = far_ld<L_CS + 2048, u32x2>(c);
          if constexpr (f == 200) cq = far_ld<L_CS + 2048 + 8, u32x2>(c);
          side(fi);
          PIN();
        });
        ST(12);
        sfor<280, 415>([&](auto fi) {
          constexpr int f = decltype(fi)::v, st = (f - 280) / 5, jn = (f - 280) % 5;
          ad[0][jn] = mm16p(W[f], B[st][0], st ? ad[0][jn] : z4);
          ahead(fi);
          PIN();
          ad[1][jn] = mm16p(W[f], B[st][1], st ? ad[1][jn] : z4);
          side(fi);
          PIN();
        });
        ST(14);
        epi(ad, 1);
        ST(15);
      }
    } else {
      LANES();
      if (wq < 2) {
        STR_INIT(24);
        lds_post(t.pa, t.pub);
        const u32 wg = OFF_WG + s * 286720;
        stream<10>(t, OFF_WO + cs * 102400 + 8 * UN, 1, 2, eb + 17, 0);
        stream<10>(t, wg, 3, 3, eb + 17, 0);
        drain(t);
        stream<10>(t, wg + 5 * UN, 8, 10, eb + 18, 1);
        stream<10>(t, wg + 3 * UN, 6, 2, eb + 29, 0);
        drain(t);
        ST(16);
        stream<20>(t, wg + 15 * UN, 3, 13, eb + 30, 1);
        ST(17);
        stream<15>(t, OFF_WD + cs * 276480, 1, 27, eb + 43, 1);
        ST(18);
        if (layer == 0) {
          stream<10>(t, OFF_WQ + s * 163840, 0, 13, 72, 1);
          stream<10>(t, OFF_WQ + s * 163840 + 13 * UN, 13, 2, 86, 0);
        } else {
          WAITV(10); lds_post(t.pa, t.seq - 2);
          WAITV(5); lds_post(t.pa, t.seq - 1);
        }
        drain(t);
        ST(19);
      } else {
        const u32 fa = L_FREE + (lane & 3) * 256;
        const u32 pa = L_FULL + g * 256 + (wq * 16 + l15) * 4;
        sfor<0, 4>([&](auto ei) { constexpr int e = decltype(ei)::v;
          spin_ge<2>(fa, eb + EV_FREE[e]);
          if (wq == 3) { if (lane < 16) sig(ws, gfl + (F_H + e) * 128, lane * 8192); }
          else { poll_ge<2>(ws, fl + (F_H + e) * 128, 16); lds_post(pa, fb + (e == 0 ? T_E1 : e == 1 ? T_E2 : e == 2 ? T_E3 : LS + T_E0) - 1); }
          ST(20 + e);
        });
        if (wq == 3) { spin_ge<2>(fa, eb + 71); lds_post(pa, fb + LS + T_KV - 1); }
      }
    }
  }
}

DEV void dma1(rsrc_t r, u32 vo, u32 ld) { asm volatile("s_mov_b32 m0, %2\n\ts_nop 0\n\tbuffer_load_dwordx4 %0, %1, 0 offen lds" :: "v"(vo), "s"(r), "s"(ld) : "memory"); }
constexpr int tile0(int s) { return (s >> 3) * 54 + ((s & 7) < 7 ? 7 * (s & 7) : 48); }
__global__ void __launch_bounds__(512) prep(const unsigned short* __restrict__ x, const unsigned short* __restrict__ wqkv, const unsigned short* __restrict__ wo,
                                            const unsigned short* __restrict__ wg, const unsigned short* __restrict__ wu, const unsigned short* __restrict__ wd,
                                            unsigned char* __restrict__ ws) {
  const int u = blockIdx.x, tid = threadIdx.x, lane = tid & 63, wv = __builtin_amdgcn_readfirstlane(tid >> 6);
  const int l31 = lane & 31, hh = lane >> 5, l15 = lane & 15, g = lane >> 4, q = l15 >> 2, p = l15 & 3;
  const u32 lb = wv * 8192;
  if (u < 256 && wv == 7) ((u32*)(ws + OFF_FLAG))[(u * 64 + lane) * 32] = 0u;
  if (u == 257 && tid < 320) *(u32x4*)(ws + OFF_ZERO + tid * 16) = u32x4{0u, 0u, 0u, 0u};
  if (u < 64) {
    u32x4 v[5];
    for (int k = 0; k < 5; ++k) v[k] = *(const u32x4*)(x + (size_t)(u * 32 + l31) * 640 + (wv * 5 + k) * 16 + hh * 8);
    float sm = 0, sq = 0;
    for (int k = 0; k < 5; ++k) {
      *(u32x4*)(ws + OFF_X + u * XT + (wv * 5 + k) * 1024 + lane * 16) = v[k];
      for (int j = 0; j < 4; ++j) { sm = dot2(v[k][j], 0x3f803f80u, sm); sq = dot2(v[k][j], v[k][j], sq); } }
    sm = swap32_add(sm); sq = swap32_add(sq);
    if (hh == 0) *(f32x2*)(ws + OFF_X + u * XT + 40960 + wv * 256 + l31 * 8) = f32x2{sm, sq};
  } else if (u < 138) {
    const bool isO = u < 84;
    const int v = isO ? u - 64 : u - 84, kh = isO ? v / 10 : v / 27, st = isO ? v % 10 : v % 27;
    const rsrc_t r = __builtin_amdgcn_make_buffer_rsrc((void*)(isO ? wo : wd), 0, isO ? 640 * 1280 : 1728 * 1280, 0x00020000);
    for (int i = 0; i < 5; ++i) { const int pc = i * 64 + lane, sl = pc / 10, e = pc % 10, gg = sl >> 3, j = sl & 7, d = 8 * (j >> 2) + 4 * (gg & 1) + (j & 3);
      int row;
      if (isO) row = st < 8 ? (kh * 8 + st) * 40 + (gg >> 1) * 20 + d : (kh * 8 + (st - 8) * 4 + gg) * 40 + (j >> 2) * 20 + 16 + (j & 3);
      else { const int s7 = (st & 3) * 2 + (gg >> 1); row = (kh * 54 + (s7 < 7 ? 7 * s7 : 48) + (st >> 2)) * 16 + d; }
      dma1(r, row * 1280 + wv * 160 + e * 16, lb + i * 1024); }
    WAITV(0);
    unsigned char* dst = ws + (isO ? OFF_WO + wv * 102400 : OFF_WD + wv * 276480) + st * UN + kh * 5120 + lane * 16;
    for (int jn = 0; jn < 5; ++jn) { const u32 a = lb + (8 * g + q) * 160 + (jn < 4 ? (jn >> 1) * 32 + 8 * p + 4 * (jn & 1) : 64 + 4 * p) * 2;
      const u32x2 lo = lds_tr(a), hi = lds_tr(a + 640);
      *(u32x4*)(dst + jn * 1024) = u32x4{lo[0], lo[1], hi[0], hi[1]}; }
  } else {
    const bool isG = u < 218;
    const int v = isG ? u - 138 : u - 218, kb = v % 5, sb = v / 5, ks = kb * 8 + wv;
    float cs[8];
    if (isG) {
      const int t0 = (sb >> 3) * 54 + ((sb & 7) < 7 ? 7 * (sb & 7) : 48), nt = (sb & 7) < 6 ? 7 : 6;
      const rsrc_t rg = __builtin_amdgcn_make_buffer_rsrc((void*)wg, 0, 640 * 3456, 0x00020000), ru = __builtin_amdgcn_make_buffer_rsrc((void*)wu, 0, 640 * 3456, 0x00020000);
      for (int i = 0; i < 4; ++i) { const int e = l15 < 2 * nt ? l15 : 0; const u32 vo = (ks * 16 + i * 4 + g) * 3456 + t0 * 32 + e * 16;
        dma1(rg, vo, lb + i * 1024); dma1(ru, vo, lb + 4096 + i * 1024); }
      WAITV(0);
      for (int t = 0; t < 7; ++t) { const u32 a = lb + (g & 1) * 4096 + (8 * hh + q) * 256 + t * 32 + p * 8;
        const u32x2 lo = lds_tr(a), hi = lds_tr(a + 1024);
        const int f = t < 3 ? (ks < 30 ? ks * 3 + t : 60 + t * 10 + ks) : t < 5 ? t * 40 + ks : ks < 20 ? 195 + 2 * ks + t : 120 + 20 * t + ks;
        *(u32x4*)(ws + OFF_WG + sb * 286720 + f * 1024 + lane * 16) = u32x4{lo[0], lo[1], hi[0], hi[1]};
        cs[t] = dot2(lo[0], 0x3f803f80u, dot2(lo[1], 0x3f803f80u, dot2(hi[0], 0x3f803f80u, dot2(hi[1], 0x3f803f80u, 0.f)))); }
      cs[7] = 0.f;
    } else {
      const rsrc_t r = __builtin_amdgcn_make_buffer_rsrc((void*)wqkv, 0, 640 * 3840, 0x00020000);
      for (int i = 0; i < 8; ++i) { const int e = l31 < 30 ? l31 : 0; dma1(r, (ks * 16 + i * 2 + hh) * 3840 + (e / 10) * 1280 + sb * 160 + (e % 10) * 16, lb + i * 1024); }
      WAITV(0);
      for (int t = 0; t < 8; ++t) { const int tt = t & 3, gam = (g & 1) * 4 + p;
        const int col = tt == 2 ? (gam < 2 ? 80 : gam < 4 ? 0 : 160) + (gam & 1) * 20 + 16 : (tt == 0 ? 80 : tt == 1 ? 160 : 0) + (g & 1) * 20 + p * 4;
        const u32 a = lb + (8 * hh + q) * 512 + ((t >> 2) * 40 + col) * 2;
        const u32x2 lo = lds_tr(a), hi = lds_tr(a + 2048);
        *(u32x4*)(ws + OFF_WQ + (sb * 2 + (t >> 2)) * 163840 + (tt < 3 ? ks * 3 + tt : 120 + ks) * 1024 + lane * 16) = u32x4{lo[0], lo[1], hi[0], hi[1]};
        cs[t] = dot2(lo[0], 0x3f803f80u, dot2(lo[1], 0x3f803f80u, dot2(hi[0], 0x3f803f80u, dot2(hi[1], 0x3f803f80u, 0.f)))); }
    }
    asm volatile("" ::: "memory");
    for (int t = 0; t < 8; ++t) lds_st<float>(lb + t * 256 + lane * 4, cs[t]);
    BAR();
    float a = 0;
    for (int i = 0; i < 8; ++i) a += lds_ld<float>(i * 8192 + wv * 256 + lane * 4);
    a = swap32_add(a);
    const int s = isG ? sb : sb * 2 + (wv >> 2), tile = isG ? 4 + wv : wv & 3;
    if (hh == 0) ((float*)(ws + OFF_CS))[(s * 5 + kb) * 384 + tile * 32 + l31] = a;
  }
}

#if defined(__HIP_DEVICE_COMPILE__)
#pragma clang attribute pop
#endif
extern "C" void kernel_launch(void* const* d_in, const int* in_sizes, int n_in, void* d_out, int out_size, void* d_ws, size_t ws_size, hipStream_t stream) {
  (void)in_sizes; (void)n_in; (void)out_size; (void)ws_size;
  (void)hipFuncSetAttribute((const void*)mega, hipFuncAttributeMaxDynamicSharedMemorySize, LDS_BYTES);
  prep<<<258, 512, 65536, stream>>>((const unsigned short*)d_in[0], (const unsigned short*)d_in[1], (const unsigned short*)d_in[2], (const unsigned short*)d_in[4], (const unsigned short*)d_in[5], (const unsigned short*)d_in[6], (unsigned char*)d_ws);
  mega<<<256, 512, LDS_BYTES, stream>>>((unsigned short*)d_out, (unsigned char*)d_ws);
}
```

```cpp
#include <hip/hip_runtime.h>
#include <stdint.h>
#if defined(__HIP_DEVICE_COMPILE__)
#pragma clang attribute push(__attribute__((target("no-packed-fp32-ops"))), apply_to = function)
#endif

typedef unsigned u32;
typedef __attribute__((ext_vector_type(4))) unsigned u32x4;
typedef __attribute__((ext_vector_type(2))) unsigned u32x2;
typedef __attribute__((ext_vector_type(8))) __bf16 bf16x8;
typedef __attribute__((ext_vector_type(2))) __bf16 bf16x2;
typedef __attribute__((ext_vector_type(4))) short s16x4;
typedef __attribute__((ext_vector_type(2))) float f32x2;
typedef __attribute__((ext_vector_type(4))) float f32x4;
typedef __attribute__((ext_vector_type(16))) float f32x16;
#define LAS __attribute__((address_space(3)))
#define GAS __attribute__((address_space(1)))
#define RLX_AGENT __ATOMIC_RELAXED, __HIP_MEMORY_SCOPE_AGENT
#define RLX_WG __ATOMIC_RELAXED, __HIP_MEMORY_SCOPE_WORKGROUP
#define DEV __device__ __forceinline__
typedef __amdgpu_buffer_rsrc_t rsrc_t;

constexpr u32 KVT = 5120;
constexpr u32 XT = 43008;
constexpr u32 HG = 55296;
constexpr u32 OFF_FLAG = 0;
constexpr u32 OFF_WQ = 2u << 20;
constexpr u32 OFF_WO = OFF_WQ + 16 * 163840;
constexpr u32 OFF_WG = OFF_WO + 8 * 102400;
constexpr u32 OFF_WD = OFF_WG + 16 * 286720;
constexpr u32 OFF_CS = OFF_WD + 8 * 276480;
constexpr u32 OFF_ZERO = OFF_CS + 16 * 5 * 1536;
constexpr u32 OFF_DUMP = OFF_ZERO + 5120;
constexpr u32 OFF_X = OFF_DUMP + 65536;
constexpr u32 XSZ = 64 * XT;
constexpr u32 OFF_KV = OFF_X + 4 * XSZ;
constexpr u32 KVSZ = 16 * 64 * KVT;
constexpr u32 OFF_O = OFF_KV + 2 * KVSZ;
constexpr u32 OSZ = 128 * 20480;
constexpr u32 OFF_H = OFF_O + 2 * OSZ;
constexpr u32 HSZ = 128 * HG;
constexpr u32 OFF_END = OFF_H + 2 * HSZ;
constexpr u32 OFF_STAMP = 96u << 20;
enum { F_X = 0, F_KV = 4, F_O = 5, F_X2 = 9, F_H = 13, F_TMO = 63 };

constexpr u32 UN = 10240;
constexpr u32 KVL = 5248;
constexpr u32 L_KV = UN;
constexpr u32 L_QH = L_KV + 12 * KVL;
constexpr u32 L_X1 = 6 * UN, L_X2 = 13 * UN;
constexpr u32 L_FULL = 15 * UN;
constexpr u32 L_FREE = L_FULL + 1024;
constexpr u32 L_CS = L_FREE + 2048;
constexpr u32 L_ATT = L_CS + 3072;
constexpr u32 LDS_BYTES = 163840;
constexpr int LF = 86, LS = 81;
constexpr int T_E0 = 49, T_E1 = 53, T_E2 = 60, T_E3 = 70, T_CS = 7, T_KV = 12, T_X2 = 75;

#define POLL_MAX 0x10000
#define SPIN_MAX 0x100000
#define BAR() do { __builtin_amdgcn_sched_barrier(0); asm volatile("s_waitcnt lgkmcnt(0)\n\ts_barrier" ::: "memory"); __builtin_amdgcn_sched_barrier(0); } while (0)
#define WAITV(n) asm volatile("s_waitcnt vmcnt(%0)" :: "i"(n) : "memory")
#define PIN() __builtin_amdgcn_sched_barrier(0)

#define ST(i) do {} while (0)
#define PST(i) do {} while (0)

DEV float asf(u32 x) { return __builtin_bit_cast(float, x); }
DEV u32 asu(float x) { return __builtin_bit_cast(u32, x); }
DEV u32 pk2(float a, float b) { f32x2 v = {a, b}; bf16x2 r = __builtin_convertvector(v, bf16x2); return __builtin_bit_cast(u32, r); }
DEV float bflo(u32 v) { return __builtin_bit_cast(float, v << 16); }
DEV float bfhi(u32 v) { return __builtin_bit_cast(float, v & 0xffff0000u); }
DEV u32x4 ld16(rsrc_t r, u32 vo, u32 so) { return __builtin_amdgcn_raw_buffer_load_b128(r, vo, so, 16); }
DEV u32x2 ld8(rsrc_t r, u32 vo, u32 so) { return __builtin_amdgcn_raw_buffer_load_b64(r, vo, so, 16); }
DEV void st16(u32x4 v, rsrc_t r, u32 vo, u32 so) { __builtin_amdgcn_raw_buffer_store_b128(v, r, vo, so, 16); }
template <typename T> DEV T lds_ld(u32 a) { return *(LAS T*)a; }
template <typename T> DEV void lds_st(u32 a, T v) { *(LAS T*)a = v; }
DEV u32x2 lds_tr(u32 a) { return __builtin_bit_cast(u32x2, __builtin_amdgcn_ds_read_tr16_b64_v4i16((LAS s16x4*)a)); }
DEV int lds_word(u32 a) { return __hip_atomic_load((LAS int*)a, RLX_WG); }
DEV void lds_post(u32 a, int v) { __hip_atomic_store((LAS int*)a, v, RLX_WG); }
DEV f32x16 mm32(u32x4 a, u32x4 b, f32x16 c) { return __builtin_amdgcn_mfma_f32_32x32x16_bf16(__builtin_bit_cast(bf16x8, a), __builtin_bit_cast(bf16x8, b), c, 0, 0, 0); }
DEV f32x16 mm32p(u32x4 a, u32x4 b, f32x16 c) { f32x16 r = mm32(a, b, c); asm volatile("" : "+v"(r)); return r; }
DEV f32x16 mm32k8(u32x2 a, u32x2 b, f32x16 c) { return __builtin_amdgcn_mfma_f32_32x32x8bf16_1k(__builtin_bit_cast(s16x4, a), __builtin_bit_cast(s16x4, b), c, 0, 0, 0); }
DEV f32x16 mm32k8p(u32x2 a, u32x2 b, f32x16 c) { f32x16 r = mm32k8(a, b, c); asm volatile("" : "+v"(r)); return r; }
DEV f32x4 mm16(u32x4 a, u32x4 b, f32x4 c) { return __builtin_amdgcn_mfma_f32_16x16x32_bf16(__builtin_bit_cast(bf16x8, a), __builtin_bit_cast(bf16x8, b), c, 0, 0, 0); }
DEV f32x4 mm16p(u32x4 a, u32x4 b, f32x4 c) { f32x4 r = mm16(a, b, c); asm volatile("" : "+v"(r)); return r; }
DEV float swap32_max(float v) { u32 x = asu(v); auto r = __builtin_amdgcn_permlane32_swap(x, x, false, false); return fmaxf(asf(r[0]), asf(r[1])); }
DEV float swap32_add(float v) { u32 x = asu(v); auto r = __builtin_amdgcn_permlane32_swap(x, x, false, false); return asf(r[0]) + asf(r[1]); }
DEV float swap16_add(float v) { u32 x = asu(v); auto r = __builtin_amdgcn_permlane16_swap(x, x, false, false); return asf(r[0]) + asf(r[1]); }
DEV float dot2(u32 a, u32 b, float c) { return __builtin_amdgcn_fdot2_f32_bf16(__builtin_bit_cast(bf16x2, a), __builtin_bit_cast(bf16x2, b), c, false); }
DEV u32 split2(float m) { const u32 ph = pk2(m, 0.f) & 0xffffu; return ph | (pk2(m - bflo(ph), 0.f) << 16); }
DEV u32 mulpk(float a, float b, float r) { u32 p, t; asm volatile("v_mul_f32 %0, %2, %4\n\tv_mul_f32 %1, %3, %4\n\tv_cvt_pk_bf16_f32 %0, %0, %1" : "=&v"(p), "=&v"(t) : "v"(a), "v"(b), "v"(r)); return p; }
DEV float geg_a(float ag, float k1, float k2) { float e; asm volatile("v_mul_f32 %0, %1, %1\n\tv_fma_f32 %0, %3, %0, %2\n\tv_mul_f32 %0, %1, %0\n\tv_exp_f32 %0, %0" : "=&v"(e) : "v"(ag), "v"(k1), "v"(k2)); return e; }
DEV float geg_b(float e, float ag, float au, float vv) { float h; asm volatile("v_fma_f32 %1, %1, %4, %4\n\tv_rcp_f32 %1, %1\n\tv_mul_f32 %0, %2, %3\n\tv_mul_f32 %0, %0, %1" : "=&v"(h), "+v"(e) : "v"(ag), "v"(au), "v"(vv)); return h; }
DEV u32 geg_c(float e, float ag, float au, float vv, float h0) { u32 h; asm volatile("v_fma_f32 %1, %1, %4, %4\n\tv_rcp_f32 %1, %1\n\tv_mul_f32 %0, %2, %3\n\tv_mul_f32 %0, %0, %1\n\tv_cvt_pk_bf16_f32 %0, %5, %0" : "=&v"(h), "+v"(e) : "v"(ag), "v"(au), "v"(vv), "v"(h0)); return h; }

DEV void dma5(rsrc_t r, u32 vo, u32 so, u32 ld) {
  u32 t;
  asm volatile("s_mov_b32 m0, %4\n\ts_add_u32 %0, %3, 0x1000\n\t"
               "buffer_load_dwordx4 %1, %2, %3 offen lds\n\t"
               "buffer_load_dwordx4 %1, %2, %3 offen offset:1024 lds\n\t"
               "buffer_load_dwordx4 %1, %2, %3 offen offset:2048 lds\n\t"
               "buffer_load_dwordx4 %1, %2, %3 offen offset:3072 lds\n\t"
               "s_add_u32 m0, m0, 0x1000\n\ts_nop 0\n\t"
               "buffer_load_dwordx4 %1, %2, %0 offen lds"
               : "=&s"(t) : "v"(vo), "s"(r), "s"(so), "s"(ld) : "memory", "scc");
}
DEV void dma5kv(rsrc_t r, u32 vo, u32 so, u32 ld) {
  u32 t;
  asm volatile("s_mov_b32 m0, %4\n\ts_add_u32 %0, %3, 0x1000\n\t"
               "buffer_load_dwordx4 %1, %2, %3 offen sc1 lds\n\t"
               "buffer_load_dwordx4 %1, %2, %3 offen offset:1024 sc1 lds\n\t"
               "buffer_load_dwordx4 %1, %2, %3 offen offset:2048 sc1 lds\n\t"
               "buffer_load_dwordx4 %1, %2, %3 offen offset:3072 sc1 lds\n\t"
               "s_add_u32 m0, m0, 0x1080\n\ts_nop 0\n\t"
               "buffer_load_dwordx4 %1, %2, %0 offen sc1 lds"
               : "=&s"(t) : "v"(vo), "s"(r), "s"(so), "s"(ld) : "memory", "scc");
}
template <typename T> DEV T anyv() { T r; asm volatile("" : "=v"(r)); return r; }
template <u32 K> DEV u32 vk() { u32 r; asm volatile("v_mov_b32 %0, %1" : "=v"(r) : "i"(K)); return r; }
#define OPQV(x) asm volatile("" : "+v"(x))
#define OPQS(x) asm volatile("" : "+s"(x))

template <int SLP> DEV void spin_ge(u32 a, int thr) {
  u32 t, n;
  asm volatile("s_mov_b32 %1, %5\n0:\n\tds_read_b32 %0, %2\n\ts_sub_u32 %1, %1, 1\n\ts_waitcnt lgkmcnt(0)\n\ts_cbranch_scc1 1f\n\tv_cmp_gt_i32 vcc, %3, %0\n\ts_cbranch_vccz 1f\n\ts_sleep %4\n\ts_branch 0b\n1:"
               : "=&v"(t), "=&s"(n) : "v"(a), "s"(thr), "i"(SLP), "i"(SPIN_MAX) : "memory", "scc", "vcc");
}
DEV void chk(int v, u32 a, int thr) { if (__builtin_expect(__builtin_amdgcn_ballot_w64(v < thr) != 0, 0)) spin_ge<0>(a, thr); }

DEV void sig(GAS unsigned char* ws, u32 so, u32 vo) { (void)__hip_atomic_fetch_add((GAS u32*)(ws + so + vo), vk<1>(), RLX_AGENT); }
template <int SLP> DEV void poll_ge(GAS unsigned char* ws, u32 so, u32 tgt) {
  GAS u32* p = (GAS u32*)(ws + so + vk<0>());
#pragma unroll 1
  for (int n = POLL_MAX; n > 0; --n) {
    if (__builtin_amdgcn_readfirstlane(__hip_atomic_load(p, RLX_AGENT)) >= tgt) break;
    if (SLP) __builtin_amdgcn_s_sleep(SLP);
  }
  __builtin_amdgcn_fence(__ATOMIC_ACQUIRE, "wavefront");
  asm volatile("" ::: "memory");
}

constexpr int PD = 4;
constexpr int ALA = 12;

template <int F> struct IC { static constexpr int v = F; };
template <int B, int E, typename Fn> DEV void sfor(Fn&& fn) { if constexpr (B < E) { fn(IC<B>{}); sfor<B + 1, E>(fn); } }

struct Ctx { u32 l16, l16h, l16t, mb; };
template <u32 OFF, typename T> DEV T far_ld(const Ctx& c) { return OFF < 65536 ? lds_ld<T>(c.l16 + OFF) : OFF < 131072 ? lds_ld<T>(c.l16h + (OFF - 65536)) : lds_ld<T>(c.l16t + (OFF - 131072)); }
DEV void freed(const Ctx& c) { (void)__builtin_amdgcn_atomic_inc32((LAS u32*)(c.mb + 1024), c.l16h, __ATOMIC_RELAXED, "workgroup"); }

constexpr int gu_tile(int f) { return f < 90 ? f % 3 : f < 120 ? (f - 90) / 10 : f < 200 ? f / 40 : f < 240 ? 5 + (f & 1) : f < 260 ? 5 : 6; }
constexpr int gu_ks(int f) { return f < 90 ? f / 3 : f < 120 ? 30 + f % 10 : f < 200 ? f % 40 : f < 240 ? (f - 200) / 2 : 20 + (f - 240) % 20; }
constexpr int gu_need(int u) { return u < 3 ? 27 + u : u < 15 ? 41 : 27 + u; }
constexpr int a_at(int f) { for (int k = ALA; k < 40; ++k) if ((k - ALA) * 5 / 2 == f) return k; return -1; }
constexpr int geg_t0(int t) { return t == 0 ? 102 : t == 1 ? 119 : t == 2 ? 136 : t == 3 ? 163 : t == 4 ? 203 : t == 5 ? 263 : 282; }
constexpr int hb_time(int b) { return b < 16 ? 217 + b * 3 / 2 : b < 32 ? 257 + (b - 16) * 2 : b < 40 ? 302 + (b - 32) * 2 : 352 + (b - 40) * 3 / 2; }
constexpr int geg_at(int tau) { for (int t = 0; t < 7; ++t) if (tau >= geg_t0(t) && tau <= geg_t0(t) + 16) return t; return -1; }
constexpr int hb_at(int tau) { for (int b = 0; b < 54; ++b) if (hb_time(b) == tau) return b; return -1; }
constexpr int T_RES = 330;
constexpr int vm_after(int t0, int t1) {
  int n = 0;
  for (int t = 0; t < 7; ++t) { const int x = geg_t0(t) + 16; if (x > t0 && x < t1) ++n; }
  for (int b = 0; b < 54; ++b) { const int x = hb_time(b); if (x >= t0 && x < t1) ++n; }
  if (T_RES >= t0 && T_RES < t1) n += 3;
  return n;
}
constexpr int EV_T[4] = {165, 205, 245, 320};
constexpr int ev_at(int tau) { for (int e = 0; e < 4; ++e) if (EV_T[e] == tau) return e; return -1; }
constexpr int EV_TILE[4] = {1, 3, 4, 6};
constexpr int EV_FREE[4] = {30 + 16, 30 + 20, 30 + 24, 58 + 8};

#define LANES() int lane; asm volatile("s_nop 2\n\tv_mbcnt_lo_u32_b32 %0, -1, 0\n\tv_mbcnt_hi_u32_b32 %0, -1, %0" : "=v"(lane)); const int l31 = lane & 31, hh = lane >> 5, l15 = lane & 15, g = lane >> 4; (void)l31; (void)hh; (void)l15; (void)g; \
  u32 pk_ = pk; OPQS(pk_); const u32 rb = pk_ & 15, s = (pk_ >> 4) & 15, wq = (pk_ >> 8) & 3, w = (pk_ >> 8) & 7, hd = w >> 2, layer = pk_ >> 12, cs = s >> 1, half = s & 1; \
  Ctx c; c.l16 = lane * 16; c.l16h = lane * 16 + 65536; c.l16t = lane * 16 + 131072; c.mb = L_FULL + wq * 256 + lane * 4; OPQV(c.l16h); OPQV(c.l16t); OPQV(c.mb); \
  const u32 gfl = OFF_FLAG + rb * 131072 + layer * 2560, fl = gfl + s * 8192; \
  const int fb = layer * LS, eb = layer * LF; \
  const u32 G = rb * 8 + half * 4 + wq, xe_s = OFF_X + (G >> 1) * XT + 5 * cs * 1024 + (G & 1) * 256, kvb = OFF_KV + layer * KVSZ + s * 64 * KVT; \
  (void)fl; (void)hd; (void)xe_s; (void)kvb; (void)fb; (void)eb;

struct Str { rsrc_t rs; u32 l16, fa, pa; int seq, pub; u32 hl; };
template <int N> DEV void stream(Str& t, u32 so, u32 lu, int n, int thr, int dthr) {
  u32 ld = lu * UN + t.hl;
  so += t.hl;
#pragma unroll 1
  for (int i = 0; i < n; ++i) {
    spin_ge<1>(t.fa, thr);
    dma5(t.rs, t.l16, so, ld);
    ++t.seq; so += UN; ld += UN; thr += dthr;
    if (ld >= 15 * UN) ld -= 15 * UN;
    WAITV(N);
    const int p = t.seq - N / 5;
    t.pub = p > t.pub ? p : t.pub;
    lds_post(t.pa, t.pub);
  }
}
DEV void drain(Str& t) { WAITV(0); t.pub = t.seq; lds_post(t.pa, t.pub); }

__global__ void __launch_bounds__(512, 2) mega(unsigned short* __restrict__ out, unsigned char* __restrict__ wsp) {
  GAS unsigned char* ws = (GAS unsigned char*)wsp;
  const int w0 = __builtin_amdgcn_readfirstlane(threadIdx.x >> 6);
  const int bx = blockIdx.x & 7, br = blockIdx.x >> 3;
  const bool isC = w0 < 4;
  u32 pk = ((bx & 3) * 4 + (br & 3)) | (((bx >> 2) * 8 + (br >> 2)) << 4) | (w0 << 8);
  const rsrc_t rs = __builtin_amdgcn_make_buffer_rsrc((void*)wsp, 0, OFF_END, 0x00020000);
#define XE_A ((g >> 1) * 1024 + (l15 + 32 * (g & 1)) * 16)
#define XE_C (4096 + (l15 + 32 * (g >> 1)) * 16)
#define STR_INIT(first) Str t; t.rs = rs; t.l16 = c.l16; t.fa = L_FREE + (lane & 7) * 256; t.pa = L_FULL + g * 256 + (wq * 16 + l15) * 4; t.seq = t.pub = fb + (first); t.hl = wq * 5120;
  {
    LANES();
    lds_post(c.mb + (isC ? 1024 : 2048), isC ? 0 : 16);
    if (!isC) { lds_post(c.mb, g < 2 ? 0 : g == 2 ? T_CS - 1 : T_KV - 1); lds_post(L_ATT + lane * 4, 0); }
    BAR();
    if (w == 4 || w == 5) { STR_INIT(0); stream<15>(t, OFF_WQ + s * 163840, 0, 15, -100, 0); drain(t); }
    if (w >= 6) {
      const u32 so = OFF_CS + s * 7680;
      for (int q = w - 6; q < (w == 6 ? 1 : 3); ++q) {
        u32x4 d;
        for (int j = 0; j < 4; ++j) { float v = 0;
          for (int i = 0; i < 5; ++i) v += asf(__builtin_amdgcn_raw_buffer_load_b32(rs, ((q * 4 + j) * 32 + l31) * 4 + i * 1536, so, 0));
          const u32 p = split2(v), x = hh ? p >> 16 : p & 0xffffu; d[j] = (q * 4 + j < 11) ? x | (x << 16) : 0u; }
        lds_st<u32x4>(L_CS + q * 1024 + c.l16, d);
      }
      if (w == 6) lds_post(L_FULL + g * 256 + (32 + l15) * 4, T_E0 - 1);
    }
  }

#pragma unroll 1
  for (; pk < 8192; pk += 4096) {
    u32x4 qm; u32x2 qr;
    ST(0);
    if (isC) {
      LANES();
      __builtin_amdgcn_s_setprio(2);
      if (layer) poll_ge<0>(ws, fl + (F_X + wq) * 128, 16);
      ST(1);
      const u32 xso = OFF_X + layer * 2 * XSZ + (rb * 4 + wq) * XT;
      u32x4 A[40], W[160];
      sfor<0, ALA>([&](auto i) { constexpr int k = decltype(i)::v; A[k] = ld16(rs, c.l16, xso + k * 1024); });
      u32x2 sp[4];
      for (int i = 0; i < 4; ++i) sp[i] = ld8(rs, hh * 1024 + l31 * 8 + i * 256, xso + 40960);
      spin_ge<0>(c.mb, fb + 1);
      f32x16 acc[3], aq;
      const f32x16 z16 = {};
      sfor<0, PD>([&](auto i) { constexpr int f = decltype(i)::v; W[f] = far_ld<f * 1024, u32x4>(c); });
      PIN();
      float rstd; u32 mo; int fv; u32x4 cq;
      u32 kp[22];
      const u32 kso = kvb + (rb * 4 + wq) * KVT;
      const float qsc = 0.22360679774997896f * 1.4426950408889634f;
      sfor<0, 160>([&](auto fi) {
        constexpr int f = decltype(fi)::v, t = f % 3, ks = f < 120 ? f / 3 : f - 120, n = f + PD;
        if constexpr (f >= 120) aq = mm32p(W[f], A[ks], aq);
        else if constexpr (ks == 0) acc[t] = mm32p(W[f], A[ks], z16); else acc[t] = mm32p(W[f], A[ks], acc[t]);
        if constexpr (n < 160) {
          if constexpr (n % 10 == 0) chk(fv, c.mb, fb + n / 10 + 1);
          W[n] = far_ld<(n % 150) * 1024, u32x4>(c);
          if constexpr (n % 10 == 9) freed(c);
          if constexpr (n % 10 == 5 && n < 150) fv = lds_word(c.mb);
        }
        if constexpr (a_at(f) >= 0) { constexpr int k = a_at(f); A[k] = ld16(rs, c.l16, xso + k * 1024); }
        if constexpr (f == 40) {
          float sm = asf(sp[0][0]) + asf(sp[1][0]) + asf(sp[2][0]) + asf(sp[3][0]), sq = asf(sp[0][1]) + asf(sp[1][1]) + asf(sp[2][1]) + asf(sp[3][1]);
          sm = swap32_add(sm); sq = swap32_add(sq);
          const float mean = sm * (1.f / 640.f);
          rstd = __builtin_amdgcn_rsqf(fmaxf(sq * (1.f / 640.f) - mean * mean, 0.f) + 1e-5f);
          mo = split2(-mean);
          OPQV(rstd); OPQV(mo);
        }
        if constexpr (f == 90) cq = far_ld<L_CS, u32x4>(c);
        if constexpr (f == 98) acc[0] = mm32k8p(u32x2{cq[0], cq[1]}, u32x2{mo, 0u}, acc[0]);
        if constexpr (f == 102) acc[1] = mm32k8p(u32x2{cq[0], cq[1]}, u32x2{0u, mo}, acc[1]);
        if constexpr (f == 106) acc[2] = mm32k8p(u32x2{cq[2], cq[3]}, u32x2{mo, 0u}, acc[2]);
        if constexpr (f == 119) aq = mm32k8p(u32x2{cq[2], cq[3]}, u32x2{0u, mo}, z16);
        if constexpr (f >= 121 && f < 132) {
          constexpr int j = (f - 121) * 2, tt = j / 8, r = (j % 8) * 2;
          if constexpr (j == 18) { const float r2 = rstd * qsc; kp[j] = mulpk(acc[2][4], acc[2][5], r2); kp[j + 1] = mulpk(acc[2][6], acc[2][7], r2); }
          else { kp[j] = mulpk(acc[tt][r], acc[tt][r + 1], rstd); kp[j + 1] = mulpk(acc[tt][r + 2], acc[tt][r + 3], rstd); }
          if constexpr (j < 16 && (j & 2)) {
            constexpr int q = j / 4;
            const u32 vo = q < 2 ? c.l16 : (2 * l31 + hh) * 16;
            const u32x4 v = {kp[j - 2], kp[j - 1], kp[j], kp[j + 1]};
            st16(v, rs, vo, kso + q * 1024);
            lds_st<u32x4>(L_KV + (8 + wq) * KVL + q * 1024 + vo, v);
          }
          if constexpr (j == 20) {
            const u32 vo = (2 * l31 + hh) * 16;
            const u32x4 v = {kp[16], kp[17], kp[20], kp[21]};
            st16(v, rs, vo, kso + 4096);
            lds_st<u32x4>(L_KV + (8 + wq) * KVL + 4224 + vo, v);
            lds_st<u32x2>(L_QH + wq * 1536 + 1024 + lane * 8, u32x2{kp[18], kp[19]});
          }
        }
        PIN();
        if constexpr (f == 152) {
          WAITV(0);
          if (c.l16 < 32 && c.l16 < (15 - rb) * 16) sig(ws, fl + F_KV * 128 + 131072, c.l16 * 8192);
          PIN();
        }
      });
      ST(4);
      {
        const float r2 = rstd * qsc;
        lds_st<u32x4>(L_QH + wq * 1536 + c.l16, u32x4{pk2(aq[8] * r2, aq[9] * r2), pk2(aq[10] * r2, aq[11] * r2), pk2(aq[12] * r2, aq[13] * r2), pk2(aq[14] * r2, aq[15] * r2)});
        qm = u32x4{pk2(aq[0] * r2, aq[1] * r2), pk2(aq[2] * r2, aq[3] * r2), pk2(aq[4] * r2, aq[5] * r2), pk2(aq[6] * r2, aq[7] * r2)};
        qr = u32x2{kp[18], kp[19]};
      }
      __builtin_amdgcn_s_setprio(0);
    } else {
      LANES();
      if (wq < 2) {
        STR_INIT(15);
        stream<0>(t, OFF_WQ + s * 163840 + 15 * UN, 0, 1, eb + 1, 0);
        stream<40>(t, OFF_WO + cs * 102400, 8, 8, eb + 9, 1);
      } else if (wq == 3) {
        spin_ge<2>(L_FREE + (lane & 3) * 256, eb + 8);
        const u32 one = ((int)(7 - rb * 4 - lane) >> 31) & 0x3f803f80u;
        if (lane < 12) lds_st<u32x2>(L_KV + lane * KVL + 4096, u32x2{one, one});
        lds_post(L_FULL + g * 256 + (48 + l15) * 4, fb + T_X2 - 1);
      }
      asm volatile("" : "=v"(qm), "=v"(qr));
    }
    {
      LANES();
      const u32 qt = hd ? 3 - wq : wq;
      f32x16 cm;
      { u32 b = (u32)(l31 - 4 * hh) << 18; OPQV(b);
        for (int r = 0; r < 16; ++r) cm[r] = asf((b - ((8 * (r >> 2) + (r & 3)) << 18)) & 0xff800000u); }
      const u32 tb = L_KV + qt * KVL;
      u32 aK = tb + hd * 1024 + c.l16;
      u32 aR = hh ? tb + 4096 : tb + 4224 + l31 * 32 + hd * 16;
      u32 aV[4];
      { const int q = l15 >> 2, p = l15 & 3;
        for (int r = 0; r < 4; ++r) { const int key = 8 * r + 4 * hh + q;
          aV[r] = !(g & 1) ? tb + 2048 + hd * 1024 + key * 32 + (p & 1) * 16 + (p >> 1) * 8 : p == 0 ? tb + 4224 + key * 32 + hd * 16 + 8 : tb + 4096; OPQV(aV[r]); } }
      OPQV(aK); OPQV(aR); OPQV(cm);
      u32 aA = L_ATT + lane * 4; OPQV(aA);
      BAR();
      if (!isC) { qm = lds_ld<u32x4>(L_QH + qt * 1536 + c.l16); qr = lds_ld<u32x2>(L_QH + qt * 1536 + 1024 + (32 + l31) * 8); }
      ST(5);
      u32x4 Km[9]; u32x2 Kr[9], Vt[9][4]; int av[9];
      auto ldk = [&](auto ii) { constexpr int i = decltype(ii)::v; Km[i] = lds_ld<u32x4>(aK + (8 - i) * KVL); Kr[i] = lds_ld<u32x2>(aR + (8 - i) * KVL); };
      auto ldv = [&](auto ii) { constexpr int i = decltype(ii)::v; for (int r = 0; r < 4; ++r) Vt[i][r] = lds_tr(aV[r] + (8 - i) * KVL); };
      ldk(IC<0>{}); av[1] = lds_word(aA); ldk(IC<1>{}); ldv(IC<0>{});
      const f32x16 z16 = {};
      f32x16 sc[9], o;
      if (hh) qr = u32x2{0u, 0u};
      sc[0] = mm32k8(Kr[0], qr, mm32(Km[0], qm, cm));
      {
        float m = fmaxf(sc[0][0], sc[0][1]);
        for (int r = 2; r < 16; r += 2) m = fmaxf(fmaxf(m, sc[0][r]), sc[0][r + 1]);
        m = swap32_max(m);
        const u32 sh = hh ? split2(-m) : 0u;
        sc[0] = mm32k8p(Kr[0], u32x2{sh, 0u}, sc[0]);
        qr[0] |= sh;
      }
      sfor<0, 9>([&](auto ii) {
        constexpr int i = decltype(ii)::v;
        if constexpr (i >= 1) {
          const int need = i <= (int)qt ? 0 : layer * 2 + (i <= (int)qt + 4 ? 1 : 2);
          if (__builtin_expect(__builtin_amdgcn_ballot_w64(av[i] < need) != 0, 0)) {
            spin_ge<1>(aA, need);
            ldk(ii); ldv(ii);
            if constexpr (i < 8) { av[i + 1] = lds_word(aA); ldk(IC<i + 1>{}); }
            if constexpr (i < 8) sc[i] = mm32k8(Kr[i], qr, mm32(Km[i], qm, z16)); else sc[i] = mm32k8(Kr[i], qr, mm32(Km[i], qm, cm));
            __builtin_amdgcn_s_waitcnt(0xC07F);
          }
        }
        if constexpr (i < 7) sc[i + 1] = mm32k8p(Kr[i + 1], qr, mm32(Km[i + 1], qm, z16));
        if constexpr (i == 7) sc[8] = mm32k8p(Kr[8], qr, mm32(Km[8], qm, cm));
        if constexpr (i < 8) ldv(IC<i + 1>{});
        if constexpr (i < 7) { av[i + 2] = lds_word(aA); ldk(IC<i + 2>{}); }
        PIN();
        float e[16];
        for (int r = 0; r < 8; ++r) e[r] = __builtin_amdgcn_exp2f(sc[i][r]);
        const u32x4 p0 = {pk2(e[0], e[1]), pk2(e[2], e[3]), pk2(e[4], e[5]), pk2(e[6], e[7])};
        const u32x4 v0 = {Vt[i][0][0], Vt[i][0][1], Vt[i][1][0], Vt[i][1][1]}, v1 = {Vt[i][2][0], Vt[i][2][1], Vt[i][3][0], Vt[i][3][1]};
        if constexpr (i == 0) o = mm32p(v0, p0, z16); else o = mm32p(v0, p0, o);
        for (int r = 8; r < 16; ++r) e[r] = __builtin_amdgcn_exp2f(sc[i][r]);
        const u32x4 p1 = {pk2(e[8], e[9]), pk2(e[10], e[11]), pk2(e[12], e[13]), pk2(e[14], e[15])};
        o = mm32p(v1, p1, o);
        PIN();
        if constexpr (i == 0) {
          for (int r = 0; r < 16; ++r) cm[r] = asf(asu(cm[r]) ^ 0xff800000u);
          OPQV(cm);
          if (w >= 6) {
            poll_ge<0>(ws, fl + F_KV * 128, rb >= 2 ? 8 : rb * 4);
            const u32 j = w == 6 ? 6 : 4, src = kvb + (rb * 4 - 8 + j) * KVT;
            dma5kv(rs, c.l16, rb >= 1 ? src + KVT : OFF_ZERO, L_KV + (j + 1) * KVL);
            dma5kv(rs, c.l16, rb >= 1 ? src : OFF_ZERO, L_KV + j * KVL);
            dma5kv(rs, c.l16, rb >= 2 ? src - 3 * KVT : OFF_ZERO, L_KV + (j - 3) * KVL);
            dma5kv(rs, c.l16, rb >= 2 ? src - 4 * KVT : OFF_ZERO, L_KV + (j - 4) * KVL);
            const u32 pa = (aA & ~128u) + (w & 1) * 128;
            WAITV(10); lds_post(pa, layer * 2 + 1);
            WAITV(0); lds_post(pa, layer * 2 + 2);
          }
          PIN();
        }
      });
      if (__builtin_amdgcn_ballot_w64(!(o[12] < 1.0e12f && o[12] > 1.0e-12f)) != 0) {
        const int gt = rb * 4 + qt, nv = gt + 1 < 9 ? gt + 1 : 9;
        if (hh) qr[0] = 0u;
        float m = -3.0e38f;
        for (int pass = 0; pass < 2; ++pass) {
          o = z16;
#pragma unroll 1
          for (int i = 0; i < nv; ++i) {
            const u32 d = (8 - i) * KVL;
            const u32 ma = (i & 7) ? 0u : ~0u, mb = i ? 0u : 0xff800000u;
            f32x16 x;
            for (int r = 0; r < 16; ++r) x[r] = asf((asu(cm[r]) & ma) ^ mb);
            x = mm32k8(lds_ld<u32x2>(aR + d), qr, mm32(lds_ld<u32x4>(aK + d), qm, x));
            if (pass == 0) { for (int r = 0; r < 16; ++r) m = fmaxf(m, x[r]); }
            else {
              for (int r = 0; r < 16; ++r) x[r] = __builtin_amdgcn_exp2f(x[r] - m);
              const u32x4 p0 = {pk2(x[0], x[1]), pk2(x[2], x[3]), pk2(x[4], x[5]), pk2(x[6], x[7])};
              const u32x4 p1 = {pk2(x[8], x[9]), pk2(x[10], x[11]), pk2(x[12], x[13]), pk2(x[14], x[15])};
              const u32x2 a = lds_tr(aV[0] + d), b = lds_tr(aV[1] + d), e = lds_tr(aV[2] + d), f = lds_tr(aV[3] + d);
              o = mm32(u32x4{a[0], a[1], b[0], b[1]}, p0, o); o = mm32(u32x4{e[0], e[1], f[0], f[1]}, p1, o);
            }
          }
          m = swap32_max(m);
        }
      }
      (void)__hip_atomic_fetch_add((LAS u32*)(c.mb + 1024 + hd * 1024), 1 + hd * (LF - 1), RLX_WG);
      ST(6);
      const float il = __builtin_amdgcn_rcpf(o[12]);
      const u32x4 om = {pk2(o[0] * il, o[1] * il), pk2(o[2] * il, o[3] * il), pk2(o[4] * il, o[5] * il), pk2(o[6] * il, o[7] * il)};
      const u32 r0 = pk2(o[8] * il, o[9] * il), r1 = pk2(o[10] * il, o[11] * il);
      const u32x4 orr = {r0, r1, (u32)__builtin_amdgcn_mov_dpp(r0, 0xf5, 0xf, 0xf, true), (u32)__builtin_amdgcn_mov_dpp(r1, 0xf5, 0xf, 0xf, true)};
      const u32 oso = OFF_O + layer * OSZ + (rb * 8 + qt * 2) * 20480 + (s >> 3) * 10240;
      const u32 tg = (l31 >> 4) * 20480;
      st16(om, rs, tg + (s & 7) * 1024 + (l15 + 16 * (2 * hd + hh)) * 16, oso);
      if (!(lane & 33)) st16(orr, rs, tg + 8192 + ((s & 7) * 2 + hd) * 128 + l15 * 8, oso);
      WAITV(0);
      if (lane < 8) sig(ws, gfl + (qt >> 1) * 8192 + (F_O + (s >> 3) + (qt & 1) * 2) * 128, lane * 16384);
      ST(7);
    }
    if (isC) {
      u32 res[10];
      auto ldres = [&](u32 xs, int l15, int g) {
        u32x4 a = ld16(rs, XE_A, xs), b = ld16(rs, XE_A + 2048, xs); u32x2 d = ld8(rs, XE_C + (g & 1) * 8, xs);
        res[0] = a[0]; res[1] = a[1]; res[2] = a[2]; res[3] = a[3]; res[4] = b[0]; res[5] = b[1]; res[6] = b[2]; res[7] = b[3]; res[8] = d[0]; res[9] = d[1]; };
      auto epi = [&](f32x4 (&acc)[2][5], int which) {
        LANES();
        const u32 xb = (which ? L_X2 : L_X1) + c.l16;
        for (int j = 0; j < 5; ++j) lds_st<f32x4>(xb + (wq ^ 1) * 5120 + j * 1024, acc[1][j]);
        freed(c);
        spin_ge<0>((c.mb ^ 256) + 1024, eb + (which ? 85 : 28));
        float v[20];
        for (int j = 0; j < 5; ++j) { f32x4 p = lds_ld<f32x4>(xb + wq * 5120 + j * 1024);
          v[4 * j + 0] = acc[0][j][0] + bflo(res[2 * j]) + p[0]; v[4 * j + 1] = acc[0][j][1] + bfhi(res[2 * j]) + p[1];
          v[4 * j + 2] = acc[0][j][2] + bflo(res[2 * j + 1]) + p[2]; v[4 * j + 3] = acc[0][j][3] + bfhi(res[2 * j + 1]) + p[3]; }
        freed(c);
        for (int j = 0; j < 10; ++j) res[j] = pk2(v[2 * j], v[2 * j + 1]);
        if (which == 1 && layer == 1) {
          GAS unsigned char* o = (GAS unsigned char*)out + (size_t)(G * 16 + l15) * 1280 + cs * 160;
          *(GAS u32x4*)(o + g * 16) = u32x4{res[0], res[1], res[2], res[3]};
          *(GAS u32x4*)(o + 64 + g * 16) = u32x4{res[4], res[5], res[6], res[7]};
          *(GAS u32x2*)(o + 128 + g * 8) = u32x2{res[8], res[9]};
        } else {
          const u32 xs = xe_s + (layer * 2 + 1 + which) * XSZ;
          st16(u32x4{res[0], res[1], res[2], res[3]}, rs, XE_A, xs);
          st16(u32x4{res[4], res[5], res[6], res[7]}, rs, XE_A + 2048, xs);
          float sm = 0, sq = 0;
          for (int j = 0; j < 10; ++j) { sm = dot2(res[j], 0x3f803f80u, sm); sq = dot2(res[j], res[j], sq); }
          sm = swap32_add(swap16_add(sm)); sq = swap32_add(swap16_add(sq));
          auto a = __builtin_amdgcn_permlane16_swap(res[8], asu(sm), false, false);
          auto b = __builtin_amdgcn_permlane16_swap(res[9], asu(sq), false, false);
          const u32x4 t = {a[0], b[0], (u32)__builtin_amdgcn_update_dpp(a[1], a[1], 0xf5, 0x2, 0xf, false), (u32)__builtin_amdgcn_update_dpp(b[1], b[1], 0xf5, 0x2, 0xf, false)};
          const u32 vo = g == 1 ? 40960 - cs * 4864 - (G & 1) * 128 + l15 * 8 : XE_C;
          if (!(g & 1) || (g == 1 && !(lane & 1))) st16(t, rs, vo, xs);
          WAITV(0);
          if (lane < 16) sig(ws, gfl + ((which ? 20 + F_X : F_X2) + half * 2 + (wq >> 1)) * 128, lane * 8192);
        }
      };
      {
        LANES();
        __builtin_amdgcn_s_setprio(2);
        const u32 kh = wq & 1, th = wq >> 1;
        ldres(xe_s + layer * 2 * XSZ, l15, g);
        Ctx k = c; k.l16 += kh * 5120; k.l16h += kh * 5120; k.l16t += kh * 5120;
        u32x4 B[10][2], W[50];
        auto wld = [&](auto ni) { constexpr int n = decltype(ni)::v; W[n] = far_ld<((8 + n / 5) % 15) * UN + (n % 5) * 1024, u32x4>(k); };
        poll_ge<0>(ws, fl + (F_O + wq) * 128, 16);
        ST(8);
        const u32 so = OFF_O + layer * OSZ + (rb * 8 + half * 4 + th * 2) * 20480 + kh * 10240;
        const u32 se[2] = {so + kh * 20480, so + (kh ^ 1) * 20480};
        u32 vr = g * 256 + l15 * 8; OPQV(vr);
        auto bld = [&](auto bi) { constexpr int b = decltype(bi)::v, st = b / 2, e = b % 2;
          if constexpr (st < 8) B[st][e] = ld16(rs, c.l16, se[e] + st * 1024);
          else { const u32x2 x = ld8(rs, vr, se[e] + st * 1024), y = ld8(rs, vr, se[e] + st * 1024 + 128); B[st][e] = u32x4{x[0], x[1], y[0], y[1]}; } };
        sfor<0, ALA>(bld);
        spin_ge<0>(c.mb, fb + 24);
        sfor<0, PD>(wld);
        PIN();
        f32x4 acc[2][5]; const f32x4 z4 = {};
        int fv;
        sfor<0, 50>([&](auto fi) {
          constexpr int f = decltype(fi)::v, st = f / 5, jn = f % 5, n = f + PD;
          acc[0][jn] = mm16p(W[f], B[st][0], st ? acc[0][jn] : z4);
          if constexpr (n < 50) {
            if constexpr (n == 40) chk(fv, c.mb, fb + 26);
            wld(IC<n>{});
            if constexpr (n % 5 == 4) freed(c);
            if constexpr (n == 35) fv = lds_word(c.mb);
          }
          PIN();
          acc[1][jn] = mm16p(W[f], B[st][1], st ? acc[1][jn] : z4);
          if constexpr (f % 2 == 0 && ALA + f / 2 < 20) bld(IC<ALA + f / 2>{});
          PIN();
        });
        ST(9);
        epi(acc, 0);
        ST(10);
      }
      {
        LANES();
        poll_ge<0>(ws, fl + (F_X2 + wq) * 128, 16);
        ST(11);
        const u32 kh = wq & 1, th = wq >> 1;
        const u32 xso = OFF_X + (layer * 2 + 1) * XSZ + (rb * 4 + wq) * XT;
        u32x4 A[40], W[415], B[27][2];
        sfor<0, ALA>([&](auto i) { constexpr int k = decltype(i)::v; A[k] = ld16(rs, c.l16, xso + k * 1024); });
        u32x2 sp[4];
        for (int i = 0; i < 4; ++i) sp[i] = ld8(rs, hh * 1024 + l31 * 8 + i * 256, xso + 40960);
        spin_ge<0>(c.mb, fb + 29);
        f32x16 acc[7];
        f32x4 ad[2][5];
        const f32x16 z16 = {}; const f32x4 z4 = {};
        sfor<0, PD>([&](auto i) { constexpr int f = decltype(i)::v; W[f] = far_ld<(30 + f) * 1024, u32x4>(c); });
        PIN();
        float k1, k2, vv, ge, gh; u32 mo, hp[4]; int fv; u32x2 cq;
        const u32 hso = OFF_H + layer * HSZ + (rb * 8 + wq * 2) * HG + (s >> 3) * 27648 + ((s >> 1) & 3) * 1024;
        u32 hvo = (l31 >> 4) * HG + (l15 + 16 * (2 * (s & 1) + hh)) * 16;
        OPQV(hvo);
        const u32 bso = OFF_H + layer * HSZ + (rb * 8 + half * 4 + th * 2) * HG + kh * 27648, bs0 = bso + kh * HG, bs1 = bso + (kh ^ 1) * HG;
        Ctx k;
        auto side = [&](auto ti) {
          constexpr int tau = decltype(ti)::v;
          if constexpr (geg_at(tau) >= 0) { constexpr int t = geg_at(tau), d = tau - geg_t0(t), j = (d / 2) & 7;
            if constexpr (d == 16) st16(u32x4{hp[0], hp[1], hp[2], hp[3]}, rs, hvo, t < 6 ? hso + t * 4096 : (s & 7) < 6 ? hso + 24576 : OFF_DUMP);
            else if constexpr (d % 2 == 0) ge = geg_a(acc[t][j], k1, k2);
            else if constexpr (j % 2 == 0) gh = geg_b(ge, acc[t][j], acc[t][8 + j], vv);
            else hp[j / 2] = geg_c(ge, acc[t][j], acc[t][8 + j], vv, gh); }
          if constexpr (hb_at(tau) >= 0) { constexpr int b = hb_at(tau), st = b / 2, e = b % 2;
            B[st][e] = ld16(rs, c.l16, (e ? bs1 : bs0) + st * 1024); }
          if constexpr (tau == T_RES) ldres(xe_s + (layer * 2 + 1) * XSZ, (c.l16 >> 4) & 15, c.l16 >> 8);
        };
        auto rel = [&](auto ti) { constexpr int tau = decltype(ti)::v;
          if constexpr (ev_at(tau) >= 0) WAITV(vm_after(geg_t0(EV_TILE[ev_at(tau)]) + 16, tau));
          freed(c); };
        auto ahead = [&](auto ti) { constexpr int tau = decltype(ti)::v, n = tau + PD;
          if constexpr (n < 280) {
            if constexpr (n % 10 == 0 && n >= 30 && gu_need(n / 10) != gu_need(n / 10 - 1)) chk(fv, c.mb, fb + gu_need(n / 10));
            W[n] = far_ld<((30 + n) % 150) * 1024, u32x4>(c);
            if constexpr (n % 10 == 9) rel(ti);
            if constexpr (n % 10 == 5) fv = lds_word(c.mb);
          } else if constexpr (n < 415) { constexpr int d = n - 280;
            if constexpr (d % 5 == 0) { chk(fv, c.mb, fb + 55 + d / 5); if constexpr (d < 130) fv = lds_word(c.mb); }
            W[n] = far_ld<((1 + d / 5) % 15) * UN + (d % 5) * 1024, u32x4>(k);
            if constexpr (d % 5 == 4) rel(ti);
          } };
        sfor<0, 280>([&](auto fi) {
          constexpr int f = decltype(fi)::v, t = gu_tile(f), ks = gu_ks(f);
          if constexpr (ks == 0 && t >= 3) acc[t] = mm32k8p(cq, (t & 1) ? u32x2{0u, mo} : u32x2{mo, 0u}, z16);
          if constexpr (ks == 0 && t < 3) acc[t] = mm32p(W[f], A[ks], z16); else acc[t] = mm32p(W[f], A[ks], acc[t]);
          if constexpr (f == 270) { k = c; k.l16 += kh * 5120; k.l16h += kh * 5120; k.l16t += kh * 5120; OPQV(k.l16); OPQV(k.l16h); OPQV(k.l16t); }
          ahead(fi);
          if constexpr (a_at(f) >= 0) { constexpr int kk = a_at(f); A[kk] = ld16(rs, c.l16, xso + kk * 1024); }
          if constexpr (f == 40) {
            float sm = asf(sp[0][0]) + asf(sp[1][0]) + asf(sp[2][0]) + asf(sp[3][0]), sq = asf(sp[0][1]) + asf(sp[1][1]) + asf(sp[2][1]) + asf(sp[3][1]);
            sm = swap32_add(sm); sq = swap32_add(sq);
            const float mean = sm * (1.f / 640.f);
            vv = fmaxf(sq * (1.f / 640.f) - mean * mean, 0.f) + 1e-5f;
            const float rstd = __builtin_amdgcn_rsqf(vv);
            k1 = -2.f * 0.7978845608028654f * 1.4426950408889634f * rstd;
            k2 = k1 * 0.044715f * rstd * rstd;
            mo = split2(-mean);
            OPQV(k1); OPQV(k2); OPQV(vv); OPQV(mo);
          }
          if constexpr (f == 70) cq = far_ld<L_CS + 1024, u32x2>(c);
          if constexpr (f == 77) acc[0] = mm32k8p(cq, u32x2{mo, 0u}, acc[0]);
          if constexpr (f == 78) { acc[1] = mm32k8p(cq, u32x2{0u, mo}, acc[1]); cq = far_ld<L_CS + 1024 + 8, u32x2>(c); }
          if constexpr (f == 85) acc[2] = mm32k8p(cq, u32x2{mo, 0u}, acc[2]);
          if constexpr (f == 112) cq = far_ld<L_CS + 1024 + 8, u32x2>(c);
          if constexpr (f == 153 || f == 193) cq = far_ld<L_CS + 2048, u32x2>(c);
          if constexpr (f == 200) cq = far_ld<L_CS + 2048 + 8, u32x2>(c);
          side(fi);
          PIN();
        });
        ST(12);
        sfor<280, 415>([&](auto fi) {
          constexpr int f = decltype(fi)::v, st = (f - 280) / 5, jn = (f - 280) % 5;
          ad[0][jn] = mm16p(W[f], B[st][0], st ? ad[0][jn] : z4);
          ahead(fi);
          PIN();
          ad[1][jn] = mm16p(W[f], B[st][1], st ? ad[1][jn] : z4);
          side(fi);
          PIN();
        });
        ST(14);
        epi(ad, 1);
        ST(15);
      }
    } else {
      LANES();
      if (wq < 2) {
        STR_INIT(24);
        lds_post(t.pa, t.pub);
        const u32 wg = OFF_WG + s * 286720;
        stream<10>(t, OFF_WO + cs * 102400 + 8 * UN, 1, 2, eb + 17, 0);
        stream<10>(t, wg, 3, 3, eb + 17, 0);
        drain(t);
        stream<10>(t, wg + 5 * UN, 8, 10, eb + 18, 1);
        stream<10>(t, wg + 3 * UN, 6, 2, eb + 29, 0);
        drain(t);
        ST(16);
        stream<20>(t, wg + 15 * UN, 3, 13, eb + 30, 1);
        ST(17);
        stream<15>(t, OFF_WD + cs * 276480, 1, 27, eb + 43, 1);
        ST(18);
        if (layer == 0) {
          stream<10>(t, OFF_WQ + s * 163840, 0, 13, 72, 1);
          stream<10>(t, OFF_WQ + s * 163840 + 13 * UN, 13, 2, 86, 0);
        } else {
          WAITV(10); lds_post(t.pa, t.seq - 2);
          WAITV(5); lds_post(t.pa, t.seq - 1);
        }
        drain(t);
        ST(19);
      } else {
        const u32 fa = L_FREE + (lane & 3) * 256;
        const u32 pa = L_FULL + g * 256 + (wq * 16 + l15) * 4;
        sfor<0, 4>([&](auto ei) { constexpr int e = decltype(ei)::v;
          spin_ge<2>(fa, eb + EV_FREE[e]);
          if (wq == 3) { if (lane < 16) sig(ws, gfl + (F_H + e) * 128, lane * 8192); }
          else { poll_ge<2>(ws, fl + (F_H + e) * 128, 16); lds_post(pa, fb + (e == 0 ? T_E1 : e == 1 ? T_E2 : e == 2 ? T_E3 : LS + T_E0) - 1); }
          ST(20 + e);
        });
        if (wq == 3) { spin_ge<2>(fa, eb + 71); lds_post(pa, fb + LS + T_KV - 1); }
      }
    }
  }
}

DEV void dma1(rsrc_t r, u32 vo, u32 ld) { asm volatile("s_mov_b32 m0, %2\n\ts_nop 0\n\tbuffer_load_dwordx4 %0, %1, 0 offen lds" :: "v"(vo), "s"(r), "s"(ld) : "memory"); }
constexpr int tile0(int s) { return (s >> 3) * 54 + ((s & 7) < 7 ? 7 * (s & 7) : 48); }
__global__ void __launch_bounds__(512) prep(const unsigned short* __restrict__ x, const unsigned short* __restrict__ wqkv, const unsigned short* __restrict__ wo,
                                            const unsigned short* __restrict__ wg, const unsigned short* __restrict__ wu, const unsigned short* __restrict__ wd,
                                            unsigned char* __restrict__ ws) {
  const int u = blockIdx.x, tid = threadIdx.x, lane = tid & 63, wv = __builtin_amdgcn_readfirstlane(tid >> 6);
  const int l31 = lane & 31, hh = lane >> 5, l15 = lane & 15, g = lane >> 4, q = l15 >> 2, p = l15 & 3;
  const u32 lb = wv * 8192;
  PST(0);
  if (u < 256 && wv == 7) ((u32*)(ws + OFF_FLAG))[(u * 64 + lane) * 32] = 0u;
  if (u == 257 && tid < 320) *(u32x4*)(ws + OFF_ZERO + tid * 16) = u32x4{0u, 0u, 0u, 0u};
  if (u < 64) {
    u32x4 v[5];
    for (int k = 0; k < 5; ++k) v[k] = *(const u32x4*)(x + (size_t)(u * 32 + l31) * 640 + (wv * 5 + k) * 16 + hh * 8);
    float sm = 0, sq = 0;
    for (int k = 0; k < 5; ++k) {
      *(u32x4*)(ws + OFF_X + u * XT + (wv * 5 + k) * 1024 + lane * 16) = v[k];
      for (int j = 0; j < 4; ++j) { sm = dot2(v[k][j], 0x3f803f80u, sm); sq = dot2(v[k][j], v[k][j], sq); } }
    sm = swap32_add(sm); sq = swap32_add(sq);
    if (hh == 0) *(f32x2*)(ws + OFF_X + u * XT + 40960 + wv * 256 + l31 * 8) = f32x2{sm, sq};
  } else if (u < 138) {
    const bool isO = u < 84;
    const int v = isO ? u - 64 : u - 84, kh = isO ? v / 10 : v / 27, st = isO ? v % 10 : v % 27;
    const rsrc_t r = __builtin_amdgcn_make_buffer_rsrc((void*)(isO ? wo : wd), 0, isO ? 640 * 1280 : 1728 * 1280, 0x00020000);
    for (int i = 0; i < 5; ++i) { const int pc = i * 64 + lane, sl = pc / 10, e = pc % 10, gg = sl >> 3, j = sl & 7, d = 8 * (j >> 2) + 4 * (gg & 1) + (j & 3);
      int row;
      if (isO) row = st < 8 ? (kh * 8 + st) * 40 + (gg >> 1) * 20 + d : (kh * 8 + (st - 8) * 4 + gg) * 40 + (j >> 2) * 20 + 16 + (j & 3);
      else { const int s7 = (st & 3) * 2 + (gg >> 1); row = (kh * 54 + (s7 < 7 ? 7 * s7 : 48) + (st >> 2)) * 16 + d; }
      dma1(r, row * 1280 + wv * 160 + e * 16, lb + i * 1024); }
    WAITV(0); PST(1);
    unsigned char* dst = ws + (isO ? OFF_WO + wv * 102400 : OFF_WD + wv * 276480) + st * UN + kh * 5120 + lane * 16;
    for (int jn = 0; jn < 5; ++jn) { const u32 a = lb + (8 * g + q) * 160 + (jn < 4 ? (jn >> 1) * 32 + 8 * p + 4 * (jn & 1) : 64 + 4 * p) * 2;
      const u32x2 lo = lds_tr(a), hi = lds_tr(a + 640);
      *(u32x4*)(dst + jn * 1024) = u32x4{lo[0], lo[1], hi[0], hi[1]}; }
  } else {
    const bool isG = u < 218;
    const int v = isG ? u - 138 : u - 218, kb = v % 5, sb = v / 5, ks = kb * 8 + wv;
    float cs[8];
    if (isG) {
      const int t0 = (sb >> 3) * 54 + ((sb & 7) < 7 ? 7 * (sb & 7) : 48), nt = (sb & 7) < 6 ? 7 : 6;
      const rsrc_t rg = __builtin_amdgcn_make_buffer_rsrc((void*)wg, 0, 640 * 3456, 0x00020000), ru = __builtin_amdgcn_make_buffer_rsrc((void*)wu, 0, 640 * 3456, 0x00020000);
      for (int i = 0; i < 4; ++i) { const int e = l15 < 2 * nt ? l15 : 0; const u32 vo = (ks * 16 + i * 4 + g) * 3456 + t0 * 32 + e * 16;
        dma1(rg, vo, lb + i * 1024); dma1(ru, vo, lb + 4096 + i * 1024); }
      WAITV(0); PST(1);
      for (int t = 0; t < 7; ++t) { const u32 a = lb + (g & 1) * 4096 + (8 * hh + q) * 256 + t * 32 + p * 8;
        const u32x2 lo = lds_tr(a), hi = lds_tr(a + 1024);
        const int f = t < 3 ? (ks < 30 ? ks * 3 + t : 60 + t * 10 + ks) : t < 5 ? t * 40 + ks : ks < 20 ? 195 + 2 * ks + t : 120 + 20 * t + ks;
        *(u32x4*)(ws + OFF_WG + sb * 286720 + f * 1024 + lane * 16) = u32x4{lo[0], lo[1], hi[0], hi[1]};
        cs[t] = dot2(lo[0], 0x3f803f80u, dot2(lo[1], 0x3f803f80u, dot2(hi[0], 0x3f803f80u, dot2(hi[1], 0x3f803f80u, 0.f)))); }
      cs[7] = 0.f;
    } else {
      const rsrc_t r = __builtin_amdgcn_make_buffer_rsrc((void*)wqkv, 0, 640 * 3840, 0x00020000);
      for (int i = 0; i < 8; ++i) { const int e = l31 < 30 ? l31 : 0; dma1(r, (ks * 16 + i * 2 + hh) * 3840 + (e / 10) * 1280 + sb * 160 + (e % 10) * 16, lb + i * 1024); }
      WAITV(0); PST(1);
      for (int t = 0; t < 8; ++t) { const int tt = t & 3, gam = (g & 1) * 4 + p;
        const int col = tt == 2 ? (gam < 2 ? 80 : gam < 4 ? 0 : 160) + (gam & 1) * 20 + 16 : (tt == 0 ? 80 : tt == 1 ? 160 : 0) + (g & 1) * 20 + p * 4;
        const u32 a = lb + (8 * hh + q) * 512 + ((t >> 2) * 40 + col) * 2;
        const u32x2 lo = lds_tr(a), hi = lds_tr(a + 2048);
        *(u32x4*)(ws + OFF_WQ + (sb * 2 + (t >> 2)) * 163840 + (tt < 3 ? ks * 3 + tt : 120 + ks) * 1024 + lane * 16) = u32x4{lo[0], lo[1], hi[0], hi[1]};
        cs[t] = dot2(lo[0], 0x3f803f80u, dot2(lo[1], 0x3f803f80u, dot2(hi[0], 0x3f803f80u, dot2(hi[1], 0x3f803f80u, 0.f)))); }
    }
    asm volatile("" ::: "memory");
    for (int t = 0; t < 8; ++t) lds_st<float>(lb + t * 256 + lane * 4, cs[t]);
    BAR();
    float a = 0;
    for (int i = 0; i < 8; ++i) a += lds_ld<float>(i * 8192 + wv * 256 + lane * 4);
    a = swap32_add(a);
    const int s = isG ? sb : sb * 2 + (wv >> 2), tile = isG ? 4 + wv : wv & 3;
    if (hh == 0) ((float*)(ws + OFF_CS))[(s * 5 + kb) * 384 + tile * 32 + l31] = a;
  }
  PST(2);
}

#if defined(__HIP_DEVICE_COMPILE__)
#pragma clang attribute pop
#endif
extern "C" void kernel_launch(void* const* d_in, const int* in_sizes, int n_in, void* d_out, int out_size, void* d_ws, size_t ws_size, hipStream_t stream) {
  (void)in_sizes; (void)n_in; (void)out_size;
  d_ws = (unsigned char*)d_ws + ((ws_size - OFF_END) & ~(size_t)0x1fffff);
  (void)hipFuncSetAttribute((const void*)mega, hipFuncAttributeMaxDynamicSharedMemorySize, LDS_BYTES);
  prep<<<258, 512, 65536, stream>>>((const unsigned short*)d_in[0], (const unsigned short*)d_in[1], (const unsigned short*)d_in[2], (const unsigned short*)d_in[4], (const unsigned short*)d_in[5], (const unsigned short*)d_in[6], (unsigned char*)d_ws);
  mega<<<256, 512, LDS_BYTES, stream>>>((unsigned short*)d_out, (unsigned char*)d_ws);
}
```

```cpp
#include <hip/hip_runtime.h>
#include <stdint.h>
#if defined(__HIP_DEVICE_COMPILE__)
#pragma clang attribute push(__attribute__((target("no-packed-fp32-ops"))), apply_to = function)
#endif

typedef unsigned u32;
typedef __attribute__((ext_vector_type(4))) unsigned u32x4;
typedef __attribute__((ext_vector_type(2))) unsigned u32x2;
typedef __attribute__((ext_vector_type(8))) __bf16 bf16x8;
typedef __attribute__((ext_vector_type(2))) __bf16 bf16x2;
typedef __attribute__((ext_vector_type(4))) short s16x4;
typedef __attribute__((ext_vector_type(2))) float f32x2;
typedef __attribute__((ext_vector_type(4))) float f32x4;
typedef __attribute__((ext_vector_type(16))) float f32x16;
#define LAS __attribute__((address_space(3)))
#define GAS __attribute__((address_space(1)))
#define RLX_AGENT __ATOMIC_RELAXED, __HIP_MEMORY_SCOPE_AGENT
#define RLX_WG __ATOMIC_RELAXED, __HIP_MEMORY_SCOPE_WORKGROUP
#define DEV __device__ __forceinline__
typedef __amdgpu_buffer_rsrc_t rsrc_t;

constexpr u32 KVT = 5120;
constexpr u32 XT = 43008;
constexpr u32 HG = 55296;
constexpr u32 OFF_FLAG = 0;
constexpr u32 OFF_WQ = 2u << 20;
constexpr u32 OFF_WO = OFF_WQ + 16 * 163840;
constexpr u32 OFF_WG = OFF_WO + 8 * 102400;
constexpr u32 OFF_WD = OFF_WG + 16 * 286720;
constexpr u32 OFF_CS = OFF_WD + 8 * 276480;
constexpr u32 OFF_ZERO = OFF_CS + 16 * 5 * 1536;
constexpr u32 OFF_DUMP = OFF_ZERO + 5120;
constexpr u32 OFF_X = OFF_DUMP + 65536;
#define REUSE 13
constexpr u32 XSZ = (REUSE & 1) ? 0 : 64 * XT;
constexpr u32 OFF_KV = OFF_X + 4 * 64 * XT;
constexpr u32 KVSZ = (REUSE & 2) ? 0 : 16 * 64 * KVT;
constexpr u32 OFF_O = OFF_KV + 2 * 16 * 64 * KVT;
constexpr u32 OSZ = (REUSE & 4) ? 0 : 128 * 20480;
constexpr u32 OFF_H = OFF_O + 2 * 128 * 20480;
constexpr u32 HSZ = (REUSE & 8) ? 0 : 128 * HG;
constexpr u32 OFF_END = OFF_H + 2 * 128 * HG;
constexpr u32 OFF_STAMP = 96u << 20;
enum { F_X = 0, F_KV = 4, F_O = 5, F_X2 = 9, F_H = 13, F_TMO = 63 };

constexpr u32 UN = 10240;
constexpr u32 KVL = 5248;
constexpr u32 L_KV = UN;
constexpr u32 L_QH = L_KV + 12 * KVL;
constexpr u32 L_X1 = 6 * UN, L_X2 = 13 * UN;
constexpr u32 L_FULL = 15 * UN;
constexpr u32 L_FREE = L_FULL + 1024;
constexpr u32 L_CS = L_FREE + 2048;
constexpr u32 L_ATT = L_CS + 3072;
constexpr u32 LDS_BYTES = 163840;
constexpr int LF = 86, LS = 81;
constexpr int T_E0 = 49, T_E1 = 53, T_E2 = 60, T_E3 = 70, T_CS = 7, T_KV = 12, T_X2 = 75;

#define POLL_MAX 0x10000
#define SPIN_MAX 0x100000
#define BAR() do { __builtin_amdgcn_sched_barrier(0); asm volatile("s_waitcnt lgkmcnt(0)\n\ts_barrier" ::: "memory"); __builtin_amdgcn_sched_barrier(0); } while (0)
#define WAITV(n) asm volatile("s_waitcnt vmcnt(%0)" :: "i"(n) : "memory")
#define PIN() __builtin_amdgcn_sched_barrier(0)

#define ST(i) do {} while (0)
#define PST(i) do {} while (0)

DEV float asf(u32 x) { return __builtin_bit_cast(float, x); }
DEV u32 asu(float x) { return __builtin_bit_cast(u32, x); }
DEV u32 pk2(float a, float b) { f32x2 v = {a, b}; bf16x2 r = __builtin_convertvector(v, bf16x2); return __builtin_bit_cast(u32, r); }
DEV float bflo(u32 v) { return __builtin_bit_cast(float, v << 16); }
DEV float bfhi(u32 v) { return __builtin_bit_cast(float, v & 0xffff0000u); }
DEV u32x4 ld16(rsrc_t r, u32 vo, u32 so) { return __builtin_amdgcn_raw_buffer_load_b128(r, vo, so, 16); }
DEV u32x2 ld8(rsrc_t r, u32 vo, u32 so) { return __builtin_amdgcn_raw_buffer_load_b64(r, vo, so, 16); }
DEV void st16(u32x4 v, rsrc_t r, u32 vo, u32 so) { __builtin_amdgcn_raw_buffer_store_b128(v, r, vo, so, 16); }
template <typename T> DEV T lds_ld(u32 a) { return *(LAS T*)a; }
template <typename T> DEV void lds_st(u32 a, T v) { *(LAS T*)a = v; }
DEV u32x2 lds_tr(u32 a) { return __builtin_bit_cast(u32x2, __builtin_amdgcn_ds_read_tr16_b64_v4i16((LAS s16x4*)a)); }
DEV int lds_word(u32 a) { return __hip_atomic_load((LAS int*)a, RLX_WG); }
DEV void lds_post(u32 a, int v) { __hip_atomic_store((LAS int*)a, v, RLX_WG); }
DEV f32x16 mm32(u32x4 a, u32x4 b, f32x16 c) { return __builtin_amdgcn_mfma_f32_32x32x16_bf16(__builtin_bit_cast(bf16x8, a), __builtin_bit_cast(bf16x8, b), c, 0, 0, 0); }
DEV f32x16 mm32p(u32x4 a, u32x4 b, f32x16 c) { f32x16 r = mm32(a, b, c); asm volatile("" : "+v"(r)); return r; }
DEV f32x16 mm32k8(u32x2 a, u32x2 b, f32x16 c) { return __builtin_amdgcn_mfma_f32_32x32x8bf16_1k(__builtin_bit_cast(s16x4, a), __builtin_bit_cast(s16x4, b), c, 0, 0, 0); }
DEV f32x16 mm32k8p(u32x2 a, u32x2 b, f32x16 c) { f32x16 r = mm32k8(a, b, c); asm volatile("" : "+v"(r)); return r; }
DEV f32x4 mm16(u32x4 a, u32x4 b, f32x4 c) { return __builtin_amdgcn_mfma_f32_16x16x32_bf16(__builtin_bit_cast(bf16x8, a), __builtin_bit_cast(bf16x8, b), c, 0, 0, 0); }
DEV f32x4 mm16p(u32x4 a, u32x4 b, f32x4 c) { f32x4 r = mm16(a, b, c); asm volatile("" : "+v"(r)); return r; }
DEV float swap32_max(float v) { u32 x = asu(v); auto r = __builtin_amdgcn_permlane32_swap(x, x, false, false); return fmaxf(asf(r[0]), asf(r[1])); }
DEV float swap32_add(float v) { u32 x = asu(v); auto r = __builtin_amdgcn_permlane32_swap(x, x, false, false); return asf(r[0]) + asf(r[1]); }
DEV float swap16_add(float v) { u32 x = asu(v); auto r = __builtin_amdgcn_permlane16_swap(x, x, false, false); return asf(r[0]) + asf(r[1]); }
DEV float dot2(u32 a, u32 b, float c) { return __builtin_amdgcn_fdot2_f32_bf16(__builtin_bit_cast(bf16x2, a), __builtin_bit_cast(bf16x2, b), c, false); }
DEV u32 split2(float m) { const u32 ph = pk2(m, 0.f) & 0xffffu; return ph | (pk2(m - bflo(ph), 0.f) << 16); }
DEV u32 mulpk(float a, float b, float r) { u32 p, t; asm volatile("v_mul_f32 %0, %2, %4\n\tv_mul_f32 %1, %3, %4\n\tv_cvt_pk_bf16_f32 %0, %0, %1" : "=&v"(p), "=&v"(t) : "v"(a), "v"(b), "v"(r)); return p; }
DEV float geg_a(float ag, float k1, float k2) { float e; asm volatile("v_mul_f32 %0, %1, %1\n\tv_fma_f32 %0, %3, %0, %2\n\tv_mul_f32 %0, %1, %0\n\tv_exp_f32 %0, %0" : "=&v"(e) : "v"(ag), "v"(k1), "v"(k2)); return e; }
DEV float geg_b(float e, float ag, float au, float vv) { float h; asm volatile("v_fma_f32 %1, %1, %4, %4\n\tv_rcp_f32 %1, %1\n\tv_mul_f32 %0, %2, %3\n\tv_mul_f32 %0, %0, %1" : "=&v"(h), "+v"(e) : "v"(ag), "v"(au), "v"(vv)); return h; }
DEV u32 geg_c(float e, float ag, float au, float vv, float h0) { u32 h; asm volatile("v_fma_f32 %1, %1, %4, %4\n\tv_rcp_f32 %1, %1\n\tv_mul_f32 %0, %2, %3\n\tv_mul_f32 %0, %0, %1\n\tv_cvt_pk_bf16_f32 %0, %5, %0" : "=&v"(h), "+v"(e) : "v"(ag), "v"(au), "v"(vv), "v"(h0)); return h; }

DEV void dma5(rsrc_t r, u32 vo, u32 so, u32 ld) {
  u32 t;
  asm volatile("s_mov_b32 m0, %4\n\ts_add_u32 %0, %3, 0x1000\n\t"
               "buffer_load_dwordx4 %1, %2, %3 offen lds\n\t"
               "buffer_load_dwordx4 %1, %2, %3 offen offset:1024 lds\n\t"
               "buffer_load_dwordx4 %1, %2, %3 offen offset:2048 lds\n\t"
               "buffer_load_dwordx4 %1, %2, %3 offen offset:3072 lds\n\t"
               "s_add_u32 m0, m0, 0x1000\n\ts_nop 0\n\t"
               "buffer_load_dwordx4 %1, %2, %0 offen lds"
               : "=&s"(t) : "v"(vo), "s"(r), "s"(so), "s"(ld) : "memory", "scc");
}
DEV void dma5kv(rsrc_t r, u32 vo, u32 so, u32 ld) {
  u32 t;
  asm volatile("s_mov_b32 m0, %4\n\ts_add_u32 %0, %3, 0x1000\n\t"
               "buffer_load_dwordx4 %1, %2, %3 offen sc1 lds\n\t"
               "buffer_load_dwordx4 %1, %2, %3 offen offset:1024 sc1 lds\n\t"
               "buffer_load_dwordx4 %1, %2, %3 offen offset:2048 sc1 lds\n\t"
               "buffer_load_dwordx4 %1, %2, %3 offen offset:3072 sc1 lds\n\t"
               "s_add_u32 m0, m0, 0x1080\n\ts_nop 0\n\t"
               "buffer_load_dwordx4 %1, %2, %0 offen sc1 lds"
               : "=&s"(t) : "v"(vo), "s"(r), "s"(so), "s"(ld) : "memory", "scc");
}
template <typename T> DEV T anyv() { T r; asm volatile("" : "=v"(r)); return r; }
template <u32 K> DEV u32 vk() { u32 r; asm volatile("v_mov_b32 %0, %1" : "=v"(r) : "i"(K)); return r; }
#define OPQV(x) asm volatile("" : "+v"(x))
#define OPQS(x) asm volatile("" : "+s"(x))

template <int SLP> DEV void spin_ge(u32 a, int thr) {
  u32 t, n;
  asm volatile("s_mov_b32 %1, %5\n0:\n\tds_read_b32 %0, %2\n\ts_sub_u32 %1, %1, 1\n\ts_waitcnt lgkmcnt(0)\n\ts_cbranch_scc1 1f\n\tv_cmp_gt_i32 vcc, %3, %0\n\ts_cbranch_vccz 1f\n\ts_sleep %4\n\ts_branch 0b\n1:"
               : "=&v"(t), "=&s"(n) : "v"(a), "s"(thr), "i"(SLP), "i"(SPIN_MAX) : "memory", "scc", "vcc");
}
DEV void chk(int v, u32 a, int thr) { if (__builtin_expect(__builtin_amdgcn_ballot_w64(v < thr) != 0, 0)) spin_ge<0>(a, thr); }

DEV void sig(GAS unsigned char* ws, u32 so, u32 vo) { (void)__hip_atomic_fetch_add((GAS u32*)(ws + so + vo), vk<1>(), RLX_AGENT); }
template <int SLP> DEV void poll_ge(GAS unsigned char* ws, u32 so, u32 tgt) {
  GAS u32* p = (GAS u32*)(ws + so + vk<0>());
#pragma unroll 1
  for (int n = POLL_MAX; n > 0; --n) {
    if (__builtin_amdgcn_readfirstlane(__hip_atomic_load(p, RLX_AGENT)) >= tgt) break;
    if (SLP) __builtin_amdgcn_s_sleep(SLP);
  }
  __builtin_amdgcn_fence(__ATOMIC_ACQUIRE, "wavefront");
  asm volatile("" ::: "memory");
}

constexpr int PD = 4;
constexpr int ALA = 12;

template <int F> struct IC { static constexpr int v = F; };
template <typename T, T... I> struct Seq {};
template <int B, typename Fn, int... I> DEV void sfor_(Fn&& fn, Seq<int, I...>) { const int d[] = {0, (fn(IC<B + I>{}), 0)...}; (void)d; }
template <int B, int E, typename Fn> DEV void sfor(Fn&& fn) { sfor_<B>(fn, __make_integer_seq<Seq, int, E - B>{}); }

struct Ctx { u32 l16, l16h, l16t, mb; };
template <u32 OFF, typename T> DEV T far_ld(const Ctx& c) { return OFF < 65536 ? lds_ld<T>(c.l16 + OFF) : OFF < 131072 ? lds_ld<T>(c.l16h + (OFF - 65536)) : lds_ld<T>(c.l16t + (OFF - 131072)); }
DEV void freed(const Ctx& c) { (void)__builtin_amdgcn_atomic_inc32((LAS u32*)(c.mb + 1024), c.l16h, __ATOMIC_RELAXED, "workgroup"); }

constexpr int gu_tile(int f) { return f < 90 ? f % 3 : f < 120 ? (f - 90) / 10 : f < 200 ? f / 40 : f < 240 ? 5 + (f & 1) : f < 260 ? 5 : 6; }
constexpr int gu_ks(int f) { return f < 90 ? f / 3 : f < 120 ? 30 + f % 10 : f < 200 ? f % 40 : f < 240 ? (f - 200) / 2 : 20 + (f - 240) % 20; }
constexpr int gu_need(int u) { return u < 3 ? 27 + u : u < 15 ? 41 : 27 + u; }
constexpr int a_at(int f) { for (int k = ALA; k < 40; ++k) if ((k - ALA) * 5 / 2 == f) return k; return -1; }
constexpr int geg_t0(int t) { return t == 0 ? 102 : t == 1 ? 119 : t == 2 ? 136 : t == 3 ? 163 : t == 4 ? 203 : t == 5 ? 263 : 282; }
constexpr int hb_time(int b) { return b < 16 ? 217 + b * 3 / 2 : b < 32 ? 257 + (b - 16) * 2 : b < 40 ? 302 + (b - 32) * 2 : 352 + (b - 40) * 3 / 2; }
constexpr int geg_at(int tau) { for (int t = 0; t < 7; ++t) if (tau >= geg_t0(t) && tau <= geg_t0(t) + 16) return t; return -1; }
constexpr int hb_at(int tau) { for (int b = 0; b < 54; ++b) if (hb_time(b) == tau) return b; return -1; }
constexpr int T_RES = 330;
constexpr int vm_after(int t0, int t1) {
  int n = 0;
  for (int t = 0; t < 7; ++t) { const int x = geg_t0(t) + 16; if (x > t0 && x < t1) ++n; }
  for (int b = 0; b < 54; ++b) { const int x = hb_time(b); if (x >= t0 && x < t1) ++n; }
  if (T_RES >= t0 && T_RES < t1) n += 3;
  return n;
}
constexpr int EV_T[4] = {165, 205, 245, 320};
constexpr int ev_at(int tau) { for (int e = 0; e < 4; ++e) if (EV_T[e] == tau) return e; return -1; }
constexpr int EV_TILE[4] = {1, 3, 4, 6};
constexpr int EV_FREE[4] = {30 + 16, 30 + 20, 30 + 24, 58 + 8};

#define LANES() int lane; asm volatile("s_nop 2\n\tv_mbcnt_lo_u32_b32 %0, -1, 0\n\tv_mbcnt_hi_u32_b32 %0, -1, %0" : "=v"(lane)); const int l31 = lane & 31, hh = lane >> 5, l15 = lane & 15, g = lane >> 4; (void)l31; (void)hh; (void)l15; (void)g; \
  u32 pk_ = pk; OPQS(pk_); const u32 rb = pk_ & 15, s = (pk_ >> 4) & 15, wq = (pk_ >> 8) & 3, w = (pk_ >> 8) & 7, hd = w >> 2, layer = pk_ >> 12, cs = s >> 1, half = s & 1; \
  Ctx c; c.l16 = lane * 16; c.l16h = lane * 16 + 65536; c.l16t = lane * 16 + 131072; c.mb = L_FULL + wq * 256 + lane * 4; OPQV(c.l16h); OPQV(c.l16t); OPQV(c.mb); \
  const u32 gfl = OFF_FLAG + rb * 131072 + layer * 2560, fl = gfl + s * 8192; \
  const int fb = layer * LS, eb = layer * LF; \
  const u32 G = rb * 8 + half * 4 + wq, xe_s = OFF_X + (G >> 1) * XT + 5 * cs * 1024 + (G & 1) * 256, kvb = OFF_KV + layer * KVSZ + s * 64 * KVT; \
  (void)fl; (void)hd; (void)xe_s; (void)kvb; (void)fb; (void)eb;

struct Str { rsrc_t rs; u32 l16, fa, pa; int seq, pub; u32 hl; };
template <int N> DEV void stream(Str& t, u32 so, u32 lu, int n, int thr, int dthr) {
  u32 ld = lu * UN + t.hl;
  so += t.hl;
#pragma unroll 1
  for (int i = 0; i < n; ++i) {
    spin_ge<1>(t.fa, thr);
    dma5(t.rs, t.l16, so, ld);
    ++t.seq; so += UN; ld += UN; thr += dthr;
    if (ld >= 15 * UN) ld -= 15 * UN;
    WAITV(N);
    const int p = t.seq - N / 5;
    t.pub = p > t.pub ? p : t.pub;
    lds_post(t.pa, t.pub);
  }
}
DEV void drain(Str& t) { WAITV(0); t.pub = t.seq; lds_post(t.pa, t.pub); }

__global__ void __launch_bounds__(512, 2) mega(unsigned short* __restrict__ out, unsigned char* __restrict__ wsp) {
  GAS unsigned char* ws = (GAS unsigned char*)wsp;
  const int w0 = __builtin_amdgcn_readfirstlane(threadIdx.x >> 6);
  const int bx = blockIdx.x & 7, br = blockIdx.x >> 3;
  const bool isC = w0 < 4;
  u32 pk = ((bx & 3) * 4 + (br & 3)) | (((bx >> 2) * 8 + (br >> 2)) << 4) | (w0 << 8);
  const rsrc_t rs = __builtin_amdgcn_make_buffer_rsrc((void*)wsp, 0, OFF_END, 0x00020000);
#define XE_A ((g >> 1) * 1024 + (l15 + 32 * (g & 1)) * 16)
#define XE_C (4096 + (l15 + 32 * (g >> 1)) * 16)
#define STR_INIT(first) Str t; t.rs = rs; t.l16 = c.l16; t.fa = L_FREE + (lane & 7) * 256; t.pa = L_FULL + g * 256 + (wq * 16 + l15) * 4; t.seq = t.pub = fb + (first); t.hl = wq * 5120;
  {
    LANES();
    lds_post(c.mb + (isC ? 1024 : 2048), isC ? 0 : 16);
    if (!isC) { lds_post(c.mb, g < 2 ? 0 : g == 2 ? T_CS - 1 : T_KV - 1); lds_post(L_ATT + lane * 4, 0); }
    BAR();
    if (w == 4 || w == 5) { STR_INIT(0); stream<15>(t, OFF_WQ + s * 163840, 0, 15, -100, 0); drain(t); }
    if (w >= 6) {
      const u32 so = OFF_CS + s * 7680;
      for (int q = w - 6; q < (w == 6 ? 1 : 3); ++q) {
        u32x4 d;
        for (int j = 0; j < 4; ++j) { float v = 0;
          for (int i = 0; i < 5; ++i) v += asf(__builtin_amdgcn_raw_buffer_load_b32(rs, ((q * 4 + j) * 32 + l31) * 4 + i * 1536, so, 0));
          const u32 p = split2(v), x = hh ? p >> 16 : p & 0xffffu; d[j] = (q * 4 + j < 11) ? x | (x << 16) : 0u; }
        lds_st<u32x4>(L_CS + q * 1024 + c.l16, d);
      }
      if (w == 6) lds_post(L_FULL + g * 256 + (32 + l15) * 4, T_E0 - 1);
    }
  }

#pragma unroll 1
  for (; pk < 8192; pk += 4096) {
    u32x4 qm; u32x2 qr;
    ST(0);
    if (isC) {
      LANES();
      __builtin_amdgcn_s_setprio(2);
      if (layer) poll_ge<0>(ws, fl + (F_X + wq) * 128, 16);
      ST(1);
      const u32 xso = OFF_X + layer * 2 * XSZ + (rb * 4 + wq) * XT;
      u32x4 A[40], W[160];
      sfor<0, ALA>([&](auto i) { constexpr int k = decltype(i)::v; A[k] = ld16(rs, c.l16, xso + k * 1024); });
      u32x2 sp[4];
      for (int i = 0; i < 4; ++i) sp[i] = ld8(rs, hh * 1024 + l31 * 8 + i * 256, xso + 40960);
      spin_ge<0>(c.mb, fb + 1);
      f32x16 acc[3], aq;
      const f32x16 z16 = {};
      sfor<0, PD>([&](auto i) { constexpr int f = decltype(i)::v; W[f] = far_ld<f * 1024, u32x4>(c); });
      PIN();
      float rstd; u32 mo; int fv; u32x4 cq;
      u32 kp[22];
      const u32 kso = kvb + (rb * 4 + wq) * KVT;
      const float qsc = 0.22360679774997896f * 1.4426950408889634f;
      sfor<0, 160>([&](auto fi) {
        constexpr int f = decltype(fi)::v, t = f % 3, ks = f < 120 ? f / 3 : f - 120, n = f + PD;
        if constexpr (f >= 120) aq = mm32p(W[f], A[ks], aq);
        else if constexpr (ks == 0) acc[t] = mm32p(W[f], A[ks], z16); else acc[t] = mm32p(W[f], A[ks], acc[t]);
        if constexpr (n < 160) {
          if constexpr (n % 10 == 0) chk(fv, c.mb, fb + n / 10 + 1);
          W[n] = far_ld<(n % 150) * 1024, u32x4>(c);
          if constexpr (n % 10 == 9) freed(c);
          if constexpr (n % 10 == 5 && n < 150) fv = lds_word(c.mb);
        }
        if constexpr (a_at(f) >= 0) { constexpr int k = a_at(f); A[k] = ld16(rs, c.l16, xso + k * 1024); }
        if constexpr (f == 40) {
          float sm = asf(sp[0][0]) + asf(sp[1][0]) + asf(sp[2][0]) + asf(sp[3][0]), sq = asf(sp[0][1]) + asf(sp[1][1]) + asf(sp[2][1]) + asf(sp[3][1]);
          sm = swap32_add(sm); sq = swap32_add(sq);
          const float mean = sm * (1.f / 640.f);
          rstd = __builtin_amdgcn_rsqf(fmaxf(sq * (1.f / 640.f) - mean * mean, 0.f) + 1e-5f);
          mo = split2(-mean);
          OPQV(rstd); OPQV(mo);
        }
        if constexpr (f == 90) cq = far_ld<L_CS, u32x4>(c);
        if constexpr (f == 98) acc[0] = mm32k8p(u32x2{cq[0], cq[1]}, u32x2{mo, 0u}, acc[0]);
        if constexpr (f == 102) acc[1] = mm32k8p(u32x2{cq[0], cq[1]}, u32x2{0u, mo}, acc[1]);
        if constexpr (f == 106) acc[2] = mm32k8p(u32x2{cq[2], cq[3]}, u32x2{mo, 0u}, acc[2]);
        if constexpr (f == 119) aq = mm32k8p(u32x2{cq[2], cq[3]}, u32x2{0u, mo}, z16);
        if constexpr (f >= 121 && f < 132) {
          constexpr int j = (f - 121) * 2, tt = j / 8, r = (j % 8) * 2;
          if constexpr (j == 18) { const float r2 = rstd * qsc; kp[j] = mulpk(acc[2][4], acc[2][5], r2); kp[j + 1] = mulpk(acc[2][6], acc[2][7], r2); }
          else { kp[j] = mulpk(acc[tt][r], acc[tt][r + 1], rstd); kp[j + 1] = mulpk(acc[tt][r + 2], acc[tt][r + 3], rstd); }
          if constexpr (j < 16 && (j & 2)) {
            constexpr int q = j / 4;
            const u32 vo = q < 2 ? c.l16 : (2 * l31 + hh) * 16;
            const u32x4 v = {kp[j - 2], kp[j - 1], kp[j], kp[j + 1]};
            st16(v, rs, vo, kso + q * 1024);
            lds_st<u32x4>(L_KV + (8 + wq) * KVL + q * 1024 + vo, v);
          }
          if constexpr (j == 20) {
            const u32 vo = (2 * l31 + hh) * 16;
            const u32x4 v = {kp[16], kp[17], kp[20], kp[21]};
            st16(v, rs, vo, kso + 4096);
            lds_st<u32x4>(L_KV + (8 + wq) * KVL + 4224 + vo, v);
            lds_st<u32x2>(L_QH + wq * 1536 + 1024 + lane * 8, u32x2{kp[18], kp[19]});
          }
        }
        PIN();
        if constexpr (f == 152) {
          WAITV(0);
          if (c.l16 < 32 && c.l16 < (15 - rb) * 16) sig(ws, fl + F_KV * 128 + 131072, c.l16 * 8192);
          PIN();
        }
      });
      ST(4);
      {
        const float r2 = rstd * qsc;
        lds_st<u32x4>(L_QH + wq * 1536 + c.l16, u32x4{pk2(aq[8] * r2, aq[9] * r2), pk2(aq[10] * r2, aq[11] * r2), pk2(aq[12] * r2, aq[13] * r2), pk2(aq[14] * r2, aq[15] * r2)});
        qm = u32x4{pk2(aq[0] * r2, aq[1] * r2), pk2(aq[2] * r2, aq[3] * r2), pk2(aq[4] * r2, aq[5] * r2), pk2(aq[6] * r2, aq[7] * r2)};
        qr = u32x2{kp[18], kp[19]};
      }
      __builtin_amdgcn_s_setprio(0);
    } else {
      LANES();
      if (wq < 2) {
        STR_INIT(15);
        stream<0>(t, OFF_WQ + s * 163840 + 15 * UN, 0, 1, eb + 1, 0);
        stream<40>(t, OFF_WO + cs * 102400, 8, 8, eb + 9, 1);
      } else if (wq == 3) {
        spin_ge<2>(L_FREE + (lane & 3) * 256, eb + 8);
        const u32 one = ((int)(7 - rb * 4 - lane) >> 31) & 0x3f803f80u;
        if (lane < 12) lds_st<u32x2>(L_KV + lane * KVL + 4096, u32x2{one, one});
        lds_post(L_FULL + g * 256 + (48 + l15) * 4, fb + T_X2 - 1);
      }
      asm volatile("" : "=v"(qm), "=v"(qr));
    }
    {
      LANES();
      const u32 qt = hd ? 3 - wq : wq;
      f32x16 cm;
      { u32 b = (u32)(l31 - 4 * hh) << 18; OPQV(b);
        for (int r = 0; r < 16; ++r) cm[r] = asf((b - ((8 * (r >> 2) + (r & 3)) << 18)) & 0xff800000u); }
      const u32 tb = L_KV + qt * KVL;
      u32 aK = tb + hd * 1024 + c.l16;
      u32 aR = hh ? tb + 4096 : tb + 4224 + l31 * 32 + hd * 16;
      u32 aV[4];
      { const int q = l15 >> 2, p = l15 & 3;
        for (int r = 0; r < 4; ++r) { const int key = 8 * r + 4 * hh + q;
          aV[r] = !(g & 1) ? tb + 2048 + hd * 1024 + key * 32 + (p & 1) * 16 + (p >> 1) * 8 : p == 0 ? tb + 4224 + key * 32 + hd * 16 + 8 : tb + 4096; OPQV(aV[r]); } }
      OPQV(aK); OPQV(aR); OPQV(cm);
      u32 aA = L_ATT + lane * 4; OPQV(aA);
      BAR();
      if (!isC) { qm = lds_ld<u32x4>(L_QH + qt * 1536 + c.l16); qr = lds_ld<u32x2>(L_QH + qt * 1536 + 1024 + (32 + l31) * 8); }
      ST(5);
      u32x4 Km[9]; u32x2 Kr[9], Vt[9][4]; int av[9];
      auto ldk = [&](auto ii) { constexpr int i = decltype(ii)::v; Km[i] = lds_ld<u32x4>(aK + (8 - i) * KVL); Kr[i] = lds_ld<u32x2>(aR + (8 - i) * KVL); };
      auto ldv = [&](auto ii) { constexpr int i = decltype(ii)::v; for (int r = 0; r < 4; ++r) Vt[i][r] = lds_tr(aV[r] + (8 - i) * KVL); };
      ldk(IC<0>{}); av[1] = lds_word(aA); ldk(IC<1>{}); ldv(IC<0>{});
      const f32x16 z16 = {};
      f32x16 sc[9], o;
      if (hh) qr = u32x2{0u, 0u};
      sc[0] = mm32k8(Kr[0], qr, mm32(Km[0], qm, cm));
      {
        float m = fmaxf(sc[0][0], sc[0][1]);
        for (int r = 2; r < 16; r += 2) m = fmaxf(fmaxf(m, sc[0][r]), sc[0][r + 1]);
        m = swap32_max(m);
        const u32 sh = hh ? split2(-m) : 0u;
        sc[0] = mm32k8p(Kr[0], u32x2{sh, 0u}, sc[0]);
        qr[0] |= sh;
      }
      sfor<0, 9>([&](auto ii) {
        constexpr int i = decltype(ii)::v;
        if constexpr (i >= 1) {
          const int need = i <= (int)qt ? 0 : layer * 2 + (i <= (int)qt + 4 ? 1 : 2);
          if (__builtin_expect(__builtin_amdgcn_ballot_w64(av[i] < need) != 0, 0)) {
            spin_ge<1>(aA, need);
            ldk(ii); ldv(ii);
            if constexpr (i < 8) { av[i + 1] = lds_word(aA); ldk(IC<i + 1>{}); }
            if constexpr (i < 8) sc[i] = mm32k8(Kr[i], qr, mm32(Km[i], qm, z16)); else sc[i] = mm32k8(Kr[i], qr, mm32(Km[i], qm, cm));
            __builtin_amdgcn_s_waitcnt(0xC07F);
          }
        }
        if constexpr (i < 7) sc[i + 1] = mm32k8p(Kr[i + 1], qr, mm32(Km[i + 1], qm, z16));
        if constexpr (i == 7) sc[8] = mm32k8p(Kr[8], qr, mm32(Km[8], qm, cm));
        if constexpr (i < 8) ldv(IC<i + 1>{});
        if constexpr (i < 7) { av[i + 2] = lds_word(aA); ldk(IC<i + 2>{}); }
        PIN();
        float e[16];
        for (int r = 0; r < 8; ++r) e[r] = __builtin_amdgcn_exp2f(sc[i][r]);
        const u32x4 p0 = {pk2(e[0], e[1]), pk2(e[2], e[3]), pk2(e[4], e[5]), pk2(e[6], e[7])};
        const u32x4 v0 = {Vt[i][0][0], Vt[i][0][1], Vt[i][1][0], Vt[i][1][1]}, v1 = {Vt[i][2][0], Vt[i][2][1], Vt[i][3][0], Vt[i][3][1]};
        if constexpr (i == 0) o = mm32p(v0, p0, z16); else o = mm32p(v0, p0, o);
        for (int r = 8; r < 16; ++r) e[r] = __builtin_amdgcn_exp2f(sc[i][r]);
        const u32x4 p1 = {pk2(e[8], e[9]), pk2(e[10], e[11]), pk2(e[12], e[13]), pk2(e[14], e[15])};
        o = mm32p(v1, p1, o);
        PIN();
        if constexpr (i == 0) {
          for (int r = 0; r < 16; ++r) cm[r] = asf(asu(cm[r]) ^ 0xff800000u);
          OPQV(cm);
          if (w >= 6) {
            poll_ge<0>(ws, fl + F_KV * 128, rb >= 2 ? 8 : rb * 4);
            const u32 j = w == 6 ? 6 : 4, src = kvb + (rb * 4 - 8 + j) * KVT;
            dma5kv(rs, c.l16, rb >= 1 ? src + KVT : OFF_ZERO, L_KV + (j + 1) * KVL);
            dma5kv(rs, c.l16, rb >= 1 ? src : OFF_ZERO, L_KV + j * KVL);
            dma5kv(rs, c.l16, rb >= 2 ? src - 3 * KVT : OFF_ZERO, L_KV + (j - 3) * KVL);
            dma5kv(rs, c.l16, rb >= 2 ? src - 4 * KVT : OFF_ZERO, L_KV + (j - 4) * KVL);
            const u32 pa = (aA & ~128u) + (w & 1) * 128;
            WAITV(10); lds_post(pa, layer * 2 + 1);
            WAITV(0); lds_post(pa, layer * 2 + 2);
          }
          PIN();
        }
      });
      if (__builtin_amdgcn_ballot_w64(!(o[12] < 1.0e12f && o[12] > 1.0e-12f)) != 0) {
        const int gt = rb * 4 + qt, nv = gt + 1 < 9 ? gt + 1 : 9;
        if (hh) qr[0] = 0u;
        float m = -3.0e38f;
        for (int pass = 0; pass < 2; ++pass) {
          o = z16;
#pragma unroll 1
          for (int i = 0; i < nv; ++i) {
            const u32 d = (8 - i) * KVL;
            const u32 ma = (i & 7) ? 0u : ~0u, mb = i ? 0u : 0xff800000u;
            f32x16 x;
            for (int r = 0; r < 16; ++r) x[r] = asf((asu(cm[r]) & ma) ^ mb);
            x = mm32k8(lds_ld<u32x2>(aR + d), qr, mm32(lds_ld<u32x4>(aK + d), qm, x));
            if (pass == 0) { for (int r = 0; r < 16; ++r) m = fmaxf(m, x[r]); }
            else {
              for (int r = 0; r < 16; ++r) x[r] = __builtin_amdgcn_exp2f(x[r] - m);
              const u32x4 p0 = {pk2(x[0], x[1]), pk2(x[2], x[3]), pk2(x[4], x[5]), pk2(x[6], x[7])};
              const u32x4 p1 = {pk2(x[8], x[9]), pk2(x[10], x[11]), pk2(x[12], x[13]), pk2(x[14], x[15])};
              const u32x2 a = lds_tr(aV[0] + d), b = lds_tr(aV[1] + d), e = lds_tr(aV[2] + d), f = lds_tr(aV[3] + d);
              o = mm32(u32x4{a[0], a[1], b[0], b[1]}, p0, o); o = mm32(u32x4{e[0], e[1], f[0], f[1]}, p1, o);
            }
          }
          m = swap32_max(m);
        }
      }
      (void)__hip_atomic_fetch_add((LAS u32*)(c.mb + 1024 + hd * 1024), 1 + hd * (LF - 1), RLX_WG);
      ST(6);
      const float il = __builtin_amdgcn_rcpf(o[12]);
      const u32x4 om = {pk2(o[0] * il, o[1] * il), pk2(o[2] * il, o[3] * il), pk2(o[4] * il, o[5] * il), pk2(o[6] * il, o[7] * il)};
      const u32 r0 = pk2(o[8] * il, o[9] * il), r1 = pk2(o[10] * il, o[11] * il);
      const u32x4 orr = {r0, r1, (u32)__builtin_amdgcn_mov_dpp(r0, 0xf5, 0xf, 0xf, true), (u32)__builtin_amdgcn_mov_dpp(r1, 0xf5, 0xf, 0xf, true)};
      const u32 oso = OFF_O + layer * OSZ + (rb * 8 + qt * 2) * 20480 + (s >> 3) * 10240;
      const u32 tg = (l31 >> 4) * 20480;
      st16(om, rs, tg + (s & 7) * 1024 + (l15 + 16 * (2 * hd + hh)) * 16, oso);
      if (!(lane & 33)) st16(orr, rs, tg + 8192 + ((s & 7) * 2 + hd) * 128 + l15 * 8, oso);
      WAITV(0);
      if (lane < 8) sig(ws, gfl + (qt >> 1) * 8192 + (F_O + (s >> 3) + (qt & 1) * 2) * 128, lane * 16384);
      ST(7);
    }
    if (isC) {
      u32 res[10];
      auto ldres = [&](u32 xs, int l15, int g) {
        u32x4 a = ld16(rs, XE_A, xs), b = ld16(rs, XE_A + 2048, xs); u32x2 d = ld8(rs, XE_C + (g & 1) * 8, xs);
        res[0] = a[0]; res[1] = a[1]; res[2] = a[2]; res[3] = a[3]; res[4] = b[0]; res[5] = b[1]; res[6] = b[2]; res[7] = b[3]; res[8] = d[0]; res[9] = d[1]; };
      auto epi = [&](f32x4 (&acc)[2][5], int which) {
        LANES();
        const u32 xb = (which ? L_X2 : L_X1) + c.l16;
        for (int j = 0; j < 5; ++j) lds_st<f32x4>(xb + (wq ^ 1) * 5120 + j * 1024, acc[1][j]);
        freed(c);
        spin_ge<0>((c.mb ^ 256) + 1024, eb + (which ? 85 : 28));
        float v[20];
        for (int j = 0; j < 5; ++j) { f32x4 p = lds_ld<f32x4>(xb + wq * 5120 + j * 1024);
          v[4 * j + 0] = acc[0][j][0] + bflo(res[2 * j]) + p[0]; v[4 * j + 1] = acc[0][j][1] + bfhi(res[2 * j]) + p[1];
          v[4 * j + 2] = acc[0][j][2] + bflo(res[2 * j + 1]) + p[2]; v[4 * j + 3] = acc[0][j][3] + bfhi(res[2 * j + 1]) + p[3]; }
        freed(c);
        for (int j = 0; j < 10; ++j) res[j] = pk2(v[2 * j], v[2 * j + 1]);
        if (which == 1 && layer == 1) {
          GAS unsigned char* o = (GAS unsigned char*)out + (size_t)(G * 16 + l15) * 1280 + cs * 160;
          *(GAS u32x4*)(o + g * 16) = u32x4{res[0], res[1], res[2], res[3]};
          *(GAS u32x4*)(o + 64 + g * 16) = u32x4{res[4], res[5], res[6], res[7]};
          *(GAS u32x2*)(o + 128 + g * 8) = u32x2{res[8], res[9]};
        } else {
          const u32 xs = xe_s + (layer * 2 + 1 + which) * XSZ;
          st16(u32x4{res[0], res[1], res[2], res[3]}, rs, XE_A, xs);
          st16(u32x4{res[4], res[5], res[6], res[7]}, rs, XE_A + 2048, xs);
          float sm = 0, sq = 0;
          for (int j = 0; j < 10; ++j) { sm = dot2(res[j], 0x3f803f80u, sm); sq = dot2(res[j], res[j], sq); }
          sm = swap32_add(swap16_add(sm)); sq = swap32_add(swap16_add(sq));
          auto a = __builtin_amdgcn_permlane16_swap(res[8], asu(sm), false, false);
          auto b = __builtin_amdgcn_permlane16_swap(res[9], asu(sq), false, false);
          const u32x4 t = {a[0], b[0], (u32)__builtin_amdgcn_update_dpp(a[1], a[1], 0xf5, 0x2, 0xf, false), (u32)__builtin_amdgcn_update_dpp(b[1], b[1], 0xf5, 0x2, 0xf, false)};
          const u32 vo = g == 1 ? 40960 - cs * 4864 - (G & 1) * 128 + l15 * 8 : XE_C;
          if (!(g & 1) || (g == 1 && !(lane & 1))) st16(t, rs, vo, xs);
          WAITV(0);
          if (lane < 16) sig(ws, gfl + ((which ? 20 + F_X : F_X2) + half * 2 + (wq >> 1)) * 128, lane * 8192);
        }
      };
      {
        LANES();
        __builtin_amdgcn_s_setprio(2);
        const u32 kh = wq & 1, th = wq >> 1;
        ldres(xe_s + layer * 2 * XSZ, l15, g);
        Ctx k = c; k.l16 += kh * 5120; k.l16h += kh * 5120; k.l16t += kh * 5120;
        u32x4 B[10][2], W[50];
        auto wld = [&](auto ni) { constexpr int n = decltype(ni)::v; W[n] = far_ld<((8 + n / 5) % 15) * UN + (n % 5) * 1024, u32x4>(k); };
        poll_ge<0>(ws, fl + (F_O + wq) * 128, 16);
        ST(8);
        const u32 so = OFF_O + layer * OSZ + (rb * 8 + half * 4 + th * 2) * 20480 + kh * 10240;
        const u32 se[2] = {so + kh * 20480, so + (kh ^ 1) * 20480};
        u32 vr = g * 256 + l15 * 8; OPQV(vr);
        auto bld = [&](auto bi) { constexpr int b = decltype(bi)::v, st = b / 2, e = b % 2;
          if constexpr (st < 8) B[st][e] = ld16(rs, c.l16, se[e] + st * 1024);
          else { const u32x2 x = ld8(rs, vr, se[e] + st * 1024), y = ld8(rs, vr, se[e] + st * 1024 + 128); B[st][e] = u32x4{x[0], x[1], y[0], y[1]}; } };
        sfor<0, ALA>(bld);
        spin_ge<0>(c.mb, fb + 24);
        sfor<0, PD>(wld);
        PIN();
        f32x4 acc[2][5]; const f32x4 z4 = {};
        int fv;
        sfor<0, 50>([&](auto fi) {
          constexpr int f = decltype(fi)::v, st = f / 5, jn = f % 5, n = f + PD;
          acc[0][jn] = mm16p(W[f], B[st][0], st ? acc[0][jn] : z4);
          if constexpr (n < 50) {
            if constexpr (n == 40) chk(fv, c.mb, fb + 26);
            wld(IC<n>{});
            if constexpr (n % 5 == 4) freed(c);
            if constexpr (n == 35) fv = lds_word(c.mb);
          }
          PIN();
          acc[1][jn] = mm16p(W[f], B[st][1], st ? acc[1][jn] : z4);
          if constexpr (f % 2 == 0 && ALA + f / 2 < 20) bld(IC<ALA + f / 2>{});
          PIN();
        });
        ST(9);
        epi(acc, 0);
        ST(10);
      }
      {
        LANES();
        poll_ge<0>(ws, fl + (F_X2 + wq) * 128, 16);
        ST(11);
        const u32 kh = wq & 1, th = wq >> 1;
        const u32 xso = OFF_X + (layer * 2 + 1) * XSZ + (rb * 4 + wq) * XT;
        u32x4 A[40], W[415], B[27][2];
        sfor<0, ALA>([&](auto i) { constexpr int k = decltype(i)::v; A[k] = ld16(rs, c.l16, xso + k * 1024); });
        u32x2 sp[4];
        for (int i = 0; i < 4; ++i) sp[i] = ld8(rs, hh * 1024 + l31 * 8 + i * 256, xso + 40960);
        spin_ge<0>(c.mb, fb + 29);
        f32x16 acc[7];
        f32x4 ad[2][5];
        const f32x16 z16 = {}; const f32x4 z4 = {};
        sfor<0, PD>([&](auto i) { constexpr int f = decltype(i)::v; W[f] = far_ld<(30 + f) * 1024, u32x4>(c); });
        PIN();
        float k1, k2, vv, ge, gh; u32 mo, hp[4]; int fv; u32x2 cq;
        const u32 hso = OFF_H + layer * HSZ + (rb * 8 + wq * 2) * HG + (s >> 3) * 27648 + ((s >> 1) & 3) * 1024;
        u32 hvo = (l31 >> 4) * HG + (l15 + 16 * (2 * (s & 1) + hh)) * 16;
        OPQV(hvo);
        const u32 bso = OFF_H + layer * HSZ + (rb * 8 + half * 4 + th * 2) * HG + kh * 27648, bs0 = bso + kh * HG, bs1 = bso + (kh ^ 1) * HG;
        Ctx k;
        auto side = [&](auto ti) {
          constexpr int tau = decltype(ti)::v;
          if constexpr (geg_at(tau) >= 0) { constexpr int t = geg_at(tau), d = tau - geg_t0(t), j = (d / 2) & 7;
            if constexpr (d == 16) st16(u32x4{hp[0], hp[1], hp[2], hp[3]}, rs, hvo, t < 6 ? hso + t * 4096 : (s & 7) < 6 ? hso + 24576 : OFF_DUMP);
            else if constexpr (d % 2 == 0) ge = geg_a(acc[t][j], k1, k2);
            else if constexpr (j % 2 == 0) gh = geg_b(ge, acc[t][j], acc[t][8 + j], vv);
            else hp[j / 2] = geg_c(ge, acc[t][j], acc[t][8 + j], vv, gh); }
          if constexpr (hb_at(tau) >= 0) { constexpr int b = hb_at(tau), st = b / 2, e = b % 2;
            B[st][e] = ld16(rs, c.l16, (e ? bs1 : bs0) + st * 1024); }
          if constexpr (tau == T_RES) ldres(xe_s + (layer * 2 + 1) * XSZ, (c.l16 >> 4) & 15, c.l16 >> 8);
        };
        auto rel = [&](auto ti) { constexpr int tau = decltype(ti)::v;
          if constexpr (ev_at(tau) >= 0) WAITV(vm_after(geg_t0(EV_TILE[ev_at(tau)]) + 16, tau));
          freed(c); };
        auto ahead = [&](auto ti) { constexpr int tau = decltype(ti)::v, n = tau + PD;
          if constexpr (n < 280) {
            if constexpr (n % 10 == 0 && n >= 30 && gu_need(n / 10) != gu_need(n / 10 - 1)) chk(fv, c.mb, fb + gu_need(n / 10));
            W[n] = far_ld<((30 + n) % 150) * 1024, u32x4>(c);
            if constexpr (n % 10 == 9) rel(ti);
            if constexpr (n % 10 == 5) fv = lds_word(c.mb);
          } else if constexpr (n < 415) { constexpr int d = n - 280;
            if constexpr (d % 5 == 0) { chk(fv, c.mb, fb + 55 + d / 5); if constexpr (d < 130) fv = lds_word(c.mb); }
            W[n] = far_ld<((1 + d / 5) % 15) * UN + (d % 5) * 1024, u32x4>(k);
            if constexpr (d % 5 == 4) rel(ti);
          } };
        sfor<0, 280>([&](auto fi) {
          constexpr int f = decltype(fi)::v, t = gu_tile(f), ks = gu_ks(f);
          if constexpr (ks == 0 && t >= 3) acc[t] = mm32k8p(cq, (t & 1) ? u32x2{0u, mo} : u32x2{mo, 0u}, z16);
          if constexpr (ks == 0 && t < 3) acc[t] = mm32p(W[f], A[ks], z16); else acc[t] = mm32p(W[f], A[ks], acc[t]);
          if constexpr (f == 270) { k = c; k.l16 += kh * 5120; k.l16h += kh * 5120; k.l16t += kh * 5120; OPQV(k.l16); OPQV(k.l16h); OPQV(k.l16t); }
          ahead(fi);
          if constexpr (a_at(f) >= 0) { constexpr int kk = a_at(f); A[kk] = ld16(rs, c.l16, xso + kk * 1024); }
          if constexpr (f == 40) {
            float sm = asf(sp[0][0]) + asf(sp[1][0]) + asf(sp[2][0]) + asf(sp[3][0]), sq = asf(sp[0][1]) + asf(sp[1][1]) + asf(sp[2][1]) + asf(sp[3][1]);
            sm = swap32_add(sm); sq = swap32_add(sq);
            const float mean = sm * (1.f / 640.f);
            vv = fmaxf(sq * (1.f / 640.f) - mean * mean, 0.f) + 1e-5f;
            const float rstd = __builtin_amdgcn_rsqf(vv);
            k1 = -2.f * 0.7978845608028654f * 1.4426950408889634f * rstd;
            k2 = k1 * 0.044715f * rstd * rstd;
            mo = split2(-mean);
            OPQV(k1); OPQV(k2); OPQV(vv); OPQV(mo);
          }
          if constexpr (f == 70) cq = far_ld<L_CS + 1024, u32x2>(c);
          if constexpr (f == 77) acc[0] = mm32k8p(cq, u32x2{mo, 0u}, acc[0]);
          if constexpr (f == 78) { acc[1] = mm32k8p(cq, u32x2{0u, mo}, acc[1]); cq = far_ld<L_CS + 1024 + 8, u32x2>(c); }
          if constexpr (f == 85) acc[2] = mm32k8p(cq, u32x2{mo, 0u}, acc[2]);
          if constexpr (f == 112) cq = far_ld<L_CS + 1024 + 8, u32x2>(c);
          if constexpr (f == 153 || f == 193) cq = far_ld<L_CS + 2048, u32x2>(c);
          if constexpr (f == 200) cq = far_ld<L_CS + 2048 + 8, u32x2>(c);
          side(fi);
          PIN();
        });
        ST(12);
        sfor<280, 415>([&](auto fi) {
          constexpr int f = decltype(fi)::v, st = (f - 280) / 5, jn = (f - 280) % 5;
          ad[0][jn] = mm16p(W[f], B[st][0], st ? ad[0][jn] : z4);
          ahead(fi);
          PIN();
          ad[1][jn] = mm16p(W[f], B[st][1], st ? ad[1][jn] : z4);
          side(fi);
          PIN();
        });
        ST(14);
        epi(ad, 1);
        ST(15);
      }
    } else {
      LANES();
      if (wq < 2) {
        STR_INIT(24);
        lds_post(t.pa, t.pub);
        const u32 wg = OFF_WG + s * 286720;
        stream<10>(t, OFF_WO + cs * 102400 + 8 * UN, 1, 2, eb + 17, 0);
        stream<10>(t, wg, 3, 3, eb + 17, 0);
        drain(t);
        stream<10>(t, wg + 5 * UN, 8, 10, eb + 18, 1);
        stream<10>(t, wg + 3 * UN, 6, 2, eb + 29, 0);
        drain(t);
        ST(16);
        stream<20>(t, wg + 15 * UN, 3, 13, eb + 30, 1);
        ST(17);
        stream<15>(t, OFF_WD + cs * 276480, 1, 27, eb + 43, 1);
        ST(18);
        if (layer == 0) {
          stream<10>(t, OFF_WQ + s * 163840, 0, 13, 72, 1);
          stream<10>(t, OFF_WQ + s * 163840 + 13 * UN, 13, 2, 86, 0);
        } else {
          WAITV(10); lds_post(t.pa, t.seq - 2);
          WAITV(5); lds_post(t.pa, t.seq - 1);
        }
        drain(t);
        ST(19);
      } else {
        const u32 fa = L_FREE + (lane & 3) * 256;
        const u32 pa = L_FULL + g * 256 + (wq * 16 + l15) * 4;
        sfor<0, 4>([&](auto ei) { constexpr int e = decltype(ei)::v;
          spin_ge<2>(fa, eb + EV_FREE[e]);
          if (wq == 3) { if (lane < 16) sig(ws, gfl + (F_H + e) * 128, lane * 8192); }
          else { poll_ge<2>(ws, fl + (F_H + e) * 128, 16); lds_post(pa, fb + (e == 0 ? T_E1 : e == 1 ? T_E2 : e == 2 ? T_E3 : LS + T_E0) - 1); }
          ST(20 + e);
        });
        if (wq == 3) { spin_ge<2>(fa, eb + 71); lds_post(pa, fb + LS + T_KV - 1); }
      }
    }
  }
}

DEV void dma1(rsrc_t r, u32 vo, u32 ld) { asm volatile("s_mov_b32 m0, %2\n\ts_nop 0\n\tbuffer_load_dwordx4 %0, %1, 0 offen lds" :: "v"(vo), "s"(r), "s"(ld) : "memory"); }
constexpr int tile0(int s) { return (s >> 3) * 54 + ((s & 7) < 7 ? 7 * (s & 7) : 48); }
__global__ void __launch_bounds__(512) prep(const unsigned short* __restrict__ x, const unsigned short* __restrict__ wqkv, const unsigned short* __restrict__ wo,
                                            const unsigned short* __restrict__ wg, const unsigned short* __restrict__ wu, const unsigned short* __restrict__ wd,
                                            unsigned char* __restrict__ ws) {
  const int u = blockIdx.x, tid = threadIdx.x, lane = tid & 63, wv = __builtin_amdgcn_readfirstlane(tid >> 6);
  const int l31 = lane & 31, hh = lane >> 5, l15 = lane & 15, g = lane >> 4, q = l15 >> 2, p = l15 & 3;
  const u32 lb = wv * 8192;
  PST(0);
  if (u < 256 && tid < 320) *(u32x4*)(ws + OFF_FLAG + u * 8192 + tid * 16) = u32x4{0u, 0u, 0u, 0u};
  if (u == 257 && tid < 320) *(u32x4*)(ws + OFF_ZERO + tid * 16) = u32x4{0u, 0u, 0u, 0u};
  if (u < 64) {
    u32x4 v[5];
    for (int k = 0; k < 5; ++k) v[k] = *(const u32x4*)(x + (size_t)(u * 32 + l31) * 640 + (wv * 5 + k) * 16 + hh * 8);
    float sm = 0, sq = 0;
    for (int k = 0; k < 5; ++k) {
      *(u32x4*)(ws + OFF_X + u * XT + (wv * 5 + k) * 1024 + lane * 16) = v[k];
      for (int j = 0; j < 4; ++j) { sm = dot2(v[k][j], 0x3f803f80u, sm); sq = dot2(v[k][j], v[k][j], sq); } }
    sm = swap32_add(sm); sq = swap32_add(sq);
    if (hh == 0) *(f32x2*)(ws + OFF_X + u * XT + 40960 + wv * 256 + l31 * 8) = f32x2{sm, sq};
  } else if (u < 138) {
    const bool isO = u < 84;
    const int v = isO ? u - 64 : u - 84, kh = isO ? v / 10 : v / 27, st = isO ? v % 10 : v % 27;
    const rsrc_t r = __builtin_amdgcn_make_buffer_rsrc((void*)(isO ? wo : wd), 0, isO ? 640 * 1280 : 1728 * 1280, 0x00020000);
    for (int i = 0; i < 5; ++i) { const int pc = i * 64 + lane, sl = pc / 10, e = pc % 10, gg = sl >> 3, j = sl & 7, d = 8 * (j >> 2) + 4 * (gg & 1) + (j & 3);
      int row;
      if (isO) row = st < 8 ? (kh * 8 + st) * 40 + (gg >> 1) * 20 + d : (kh * 8 + (st - 8) * 4 + gg) * 40 + (j >> 2) * 20 + 16 + (j & 3);
      else { const int s7 = (st & 3) * 2 + (gg >> 1); row = (kh * 54 + (s7 < 7 ? 7 * s7 : 48) + (st >> 2)) * 16 + d; }
      dma1(r, row * 1280 + wv * 160 + e * 16, lb + i * 1024); }
    WAITV(0); PST(1);
    unsigned char* dst = ws + (isO ? OFF_WO + wv * 102400 : OFF_WD + wv * 276480) + st * UN + kh * 5120 + lane * 16;
    for (int jn = 0; jn < 5; ++jn) { const u32 a = lb + (8 * g + q) * 160 + (jn < 4 ? (jn >> 1) * 32 + 8 * p + 4 * (jn & 1) : 64 + 4 * p) * 2;
      const u32x2 lo = lds_tr(a), hi = lds_tr(a + 640);
      *(u32x4*)(dst + jn * 1024) = u32x4{lo[0], lo[1], hi[0], hi[1]}; }
  } else {
    const bool isG = u < 218;
    const int v = isG ? u - 138 : u - 218, kb = v % 5, sb = v / 5, ks = kb * 8 + wv;
    float cs[8];
    if (isG) {
      const int t0 = (sb >> 3) * 54 + ((sb & 7) < 7 ? 7 * (sb & 7) : 48), nt = (sb & 7) < 6 ? 7 : 6;
      const rsrc_t rg = __builtin_amdgcn_make_buffer_rsrc((void*)wg, 0, 640 * 3456, 0x00020000), ru = __builtin_amdgcn_make_buffer_rsrc((void*)wu, 0, 640 * 3456, 0x00020000);
      for (int i = 0; i < 4; ++i) { const int e = l15 < 2 * nt ? l15 : 0; const u32 vo = (ks * 16 + i * 4 + g) * 3456 + t0 * 32 + e * 16;
        dma1(rg, vo, lb + i * 1024); dma1(ru, vo, lb + 4096 + i * 1024); }
      WAITV(0); PST(1);
      for (int t = 0; t < 7; ++t) { const u32 a = lb + (g & 1) * 4096 + (8 * hh + q) * 256 + t * 32 + p * 8;
        const u32x2 lo = lds_tr(a), hi = lds_tr(a + 1024);
        const int f = t < 3 ? (ks < 30 ? ks * 3 + t : 60 + t * 10 + ks) : t < 5 ? t * 40 + ks : ks < 20 ? 195 + 2 * ks + t : 120 + 20 * t + ks;
        *(u32x4*)(ws + OFF_WG + sb * 286720 + f * 1024 + lane * 16) = u32x4{lo[0], lo[1], hi[0], hi[1]};
        cs[t] = dot2(lo[0], 0x3f803f80u, dot2(lo[1], 0x3f803f80u, dot2(hi[0], 0x3f803f80u, dot2(hi[1], 0x3f803f80u, 0.f)))); }
      cs[7] = 0.f;
    } else {
      const rsrc_t r = __builtin_amdgcn_make_buffer_rsrc((void*)wqkv, 0, 640 * 3840, 0x00020000);
      for (int i = 0; i < 8; ++i) { const int e = l31 < 30 ? l31 : 0; dma1(r, (ks * 16 + i * 2 + hh) * 3840 + (e / 10) * 1280 + sb * 160 + (e % 10) * 16, lb + i * 1024); }
      WAITV(0); PST(1);
      for (int t = 0; t < 8; ++t) { const int tt = t & 3, gam = (g & 1) * 4 + p;
        const int col = tt == 2 ? (gam < 2 ? 80 : gam < 4 ? 0 : 160) + (gam & 1) * 20 + 16 : (tt == 0 ? 80 : tt == 1 ? 160 : 0) + (g & 1) * 20 + p * 4;
        const u32 a = lb + (8 * hh + q) * 512 + ((t >> 2) * 40 + col) * 2;
        const u32x2 lo = lds_tr(a), hi = lds_tr(a + 2048);
        *(u32x4*)(ws + OFF_WQ + (sb * 2 + (t >> 2)) * 163840 + (tt < 3 ? ks * 3 + tt : 120 + ks) * 1024 + lane * 16) = u32x4{lo[0], lo[1], hi[0], hi[1]};
        cs[t] = dot2(lo[0], 0x3f803f80u, dot2(lo[1], 0x3f803f80u, dot2(hi[0], 0x3f803f80u, dot2(hi[1], 0x3f803f80u, 0.f)))); }
    }
    asm volatile("" ::: "memory");
    for (int t = 0; t < 8; ++t) lds_st<float>(lb + t * 256 + lane * 4, cs[t]);
    BAR();
    float a = 0;
    for (int i = 0; i < 8; ++i) a += lds_ld<float>(i * 8192 + wv * 256 + lane * 4);
    a = swap32_add(a);
    const int s = isG ? sb : sb * 2 + (wv >> 2), tile = isG ? 4 + wv : wv & 3;
    if (hh == 0) ((float*)(ws + OFF_CS))[(s * 5 + kb) * 384 + tile * 32 + l31] = a;
  }
  PST(2);
}

#if defined(__HIP_DEVICE_COMPILE__)
#pragma clang attribute pop
#endif
extern "C" void kernel_launch(void* const* d_in, const int* in_sizes, int n_in, void* d_out, int out_size, void* d_ws, size_t ws_size, hipStream_t stream) {
  (void)in_sizes; (void)n_in; (void)out_size; (void)ws_size;
  (void)hipFuncSetAttribute((const void*)mega, hipFuncAttributeMaxDynamicSharedMemorySize, LDS_BYTES);
  prep<<<258, 512, 65536, stream>>>((const unsigned short*)d_in[0], (const unsigned short*)d_in[1], (const unsigned short*)d_in[2], (const unsigned short*)d_in[4], (const unsigned short*)d_in[5], (const unsigned short*)d_in[6], (unsigned char*)d_ws);
  mega<<<256, 512, LDS_BYTES, stream>>>((unsigned short*)d_out, (unsigned char*)d_ws);
}
```

```cpp
#include <hip/hip_runtime.h>
#include <stdint.h>
#if defined(__HIP_DEVICE_COMPILE__)
#pragma clang attribute push(__attribute__((target("no-packed-fp32-ops"))), apply_to = function)
#endif

typedef unsigned u32;
typedef __attribute__((ext_vector_type(4))) unsigned u32x4;
typedef __attribute__((ext_vector_type(2))) unsigned u32x2;
typedef __attribute__((ext_vector_type(8))) __bf16 bf16x8;
typedef __attribute__((ext_vector_type(2))) __bf16 bf16x2;
typedef __attribute__((ext_vector_type(4))) short s16x4;
typedef __attribute__((ext_vector_type(2))) float f32x2;
typedef __attribute__((ext_vector_type(4))) float f32x4;
typedef __attribute__((ext_vector_type(16))) float f32x16;
#define LAS __attribute__((address_space(3)))
#define GAS __attribute__((address_space(1)))
#define RLX_AGENT __ATOMIC_RELAXED, __HIP_MEMORY_SCOPE_AGENT
#define RLX_WG __ATOMIC_RELAXED, __HIP_MEMORY_SCOPE_WORKGROUP
#define DEV __device__ __forceinline__
typedef __amdgpu_buffer_rsrc_t rsrc_t;

constexpr u32 KVT = 5120;
constexpr u32 XT = 43008;
constexpr u32 HG = 55296;
constexpr u32 OFF_FLAG = 0;
constexpr u32 OFF_WQ = 2u << 20;
constexpr u32 OFF_WO = OFF_WQ + 16 * 163840;
constexpr u32 OFF_WG = OFF_WO + 8 * 102400;
constexpr u32 OFF_WD = OFF_WG + 16 * 286720;
constexpr u32 OFF_CS = OFF_WD + 8 * 276480;
constexpr u32 OFF_ZERO = OFF_CS + 16 * 5 * 1536;
constexpr u32 OFF_SEQ = OFF_ZERO + 5120;
constexpr u32 OFF_DUMP = OFF_SEQ + 1024;
constexpr u32 OFF_X = OFF_DUMP + 65536;
#define REUSE 0
constexpr u32 XSZ = (REUSE & 1) ? 0 : 64 * XT;
constexpr u32 OFF_KV = OFF_X + 4 * 64 * XT;
constexpr u32 KVSZ = (REUSE & 2) ? 0 : 16 * 64 * KVT;
constexpr u32 OFF_O = OFF_KV + 2 * 16 * 64 * KVT;
constexpr u32 OSZ = (REUSE & 4) ? 0 : 128 * 20480;
constexpr u32 OFF_H = OFF_O + 2 * 128 * 20480;
constexpr u32 HSZ = (REUSE & 8) ? 0 : 128 * HG;
constexpr u32 OFF_END = OFF_H + 2 * 128 * HG;
constexpr u32 OFF_STAMP = 96u << 20;
enum { F_X = 0, F_KV = 4, F_O = 5, F_X2 = 9, F_H = 13, F_TMO = 63 };

constexpr u32 UN = 10240;
constexpr u32 KVL = 5248;
constexpr u32 L_KV = UN;
constexpr u32 L_QH = L_KV + 12 * KVL;
constexpr u32 L_X1 = 6 * UN, L_X2 = 13 * UN;
constexpr u32 L_FULL = 15 * UN;
constexpr u32 L_FREE = L_FULL + 1024;
constexpr u32 L_CS = L_FREE + 2048;
constexpr u32 L_ATT = L_CS + 3072;
constexpr u32 LDS_BYTES = 163840;
constexpr int LF = 86, LS = 81;
constexpr int T_CS = 7, T_KV = 12, T_X2 = 81;

#define POLL_MAX 0x10000
#define SPIN_MAX 0x100000
#define BAR() do { __builtin_amdgcn_sched_barrier(0); asm volatile("s_waitcnt lgkmcnt(0)\n\ts_barrier" ::: "memory"); __builtin_amdgcn_sched_barrier(0); } while (0)
#define WAITV(n) asm volatile("s_waitcnt vmcnt(%0)" :: "i"(n) : "memory")
#define PIN() __builtin_amdgcn_sched_barrier(0)

#define ST(i) do {} while (0)
#define PST(i) do {} while (0)
#define STQ(k) do {} while (0)
#define STF(k, i) do {} while (0)

DEV float asf(u32 x) { return __builtin_bit_cast(float, x); }
DEV u32 asu(float x) { return __builtin_bit_cast(u32, x); }
DEV u32 pk2(float a, float b) { f32x2 v = {a, b}; bf16x2 r = __builtin_convertvector(v, bf16x2); return __builtin_bit_cast(u32, r); }
DEV float bflo(u32 v) { return __builtin_bit_cast(float, v << 16); }
DEV float bfhi(u32 v) { return __builtin_bit_cast(float, v & 0xffff0000u); }
DEV u32x4 ld16(rsrc_t r, u32 vo, u32 so) { return __builtin_amdgcn_raw_buffer_load_b128(r, vo, so, 16); }
DEV u32x2 ld8(rsrc_t r, u32 vo, u32 so) { return __builtin_amdgcn_raw_buffer_load_b64(r, vo, so, 16); }
DEV void st16(u32x4 v, rsrc_t r, u32 vo, u32 so) { __builtin_amdgcn_raw_buffer_store_b128(v, r, vo, so, 16); }
template <typename T> DEV T lds_ld(u32 a) { return *(LAS T*)a; }
template <typename T> DEV void lds_st(u32 a, T v) { *(LAS T*)a = v; }
DEV u32x2 lds_tr(u32 a) { return __builtin_bit_cast(u32x2, __builtin_amdgcn_ds_read_tr16_b64_v4i16((LAS s16x4*)a)); }
DEV int lds_word(u32 a) { return __hip_atomic_load((LAS int*)a, RLX_WG); }
DEV void lds_post(u32 a, int v) { __hip_atomic_store((LAS int*)a, v, RLX_WG); }
DEV f32x16 mm32(u32x4 a, u32x4 b, f32x16 c) { return __builtin_amdgcn_mfma_f32_32x32x16_bf16(__builtin_bit_cast(bf16x8, a), __builtin_bit_cast(bf16x8, b), c, 0, 0, 0); }
DEV f32x16 mm32p(u32x4 a, u32x4 b, f32x16 c) { f32x16 r = mm32(a, b, c); asm volatile("" : "+v"(r)); return r; }
DEV f32x16 mm32k8(u32x2 a, u32x2 b, f32x16 c) { return __builtin_amdgcn_mfma_f32_32x32x8bf16_1k(__builtin_bit_cast(s16x4, a), __builtin_bit_cast(s16x4, b), c, 0, 0, 0); }
DEV f32x16 mm32k8p(u32x2 a, u32x2 b, f32x16 c) { f32x16 r = mm32k8(a, b, c); asm volatile("" : "+v"(r)); return r; }
DEV f32x4 mm16(u32x4 a, u32x4 b, f32x4 c) { return __builtin_amdgcn_mfma_f32_16x16x32_bf16(__builtin_bit_cast(bf16x8, a), __builtin_bit_cast(bf16x8, b), c, 0, 0, 0); }
DEV f32x4 mm16p(u32x4 a, u32x4 b, f32x4 c) { f32x4 r = mm16(a, b, c); asm volatile("" : "+v"(r)); return r; }
DEV float swap32_max(float v) { u32 x = asu(v); auto r = __builtin_amdgcn_permlane32_swap(x, x, false, false); return fmaxf(asf(r[0]), asf(r[1])); }
DEV float swap32_add(float v) { u32 x = asu(v); auto r = __builtin_amdgcn_permlane32_swap(x, x, false, false); return asf(r[0]) + asf(r[1]); }
DEV float swap16_add(float v) { u32 x = asu(v); auto r = __builtin_amdgcn_permlane16_swap(x, x, false, false); return asf(r[0]) + asf(r[1]); }
DEV float dot2(u32 a, u32 b, float c) { return __builtin_amdgcn_fdot2_f32_bf16(__builtin_bit_cast(bf16x2, a), __builtin_bit_cast(bf16x2, b), c, false); }
DEV u32 split2(float m) { const u32 ph = pk2(m, 0.f) & 0xffffu; return ph | (pk2(m - bflo(ph), 0.f) << 16); }
DEV u32 mulpk(float a, float b, float r) { u32 p, t; asm volatile("v_mul_f32 %0, %2, %4\n\tv_mul_f32 %1, %3, %4\n\tv_cvt_pk_bf16_f32 %0, %0, %1" : "=&v"(p), "=&v"(t) : "v"(a), "v"(b), "v"(r)); return p; }
DEV float xmul(float a, float b) { float r; asm volatile("v_mul_f32 %0, %1, %2" : "=v"(r) : "v"(a), "v"(b)); return r; }
DEV float xfma(float a, float b, float c) { float r; asm volatile("v_fma_f32 %0, %1, %2, %3" : "=v"(r) : "v"(a), "v"(b), "v"(c)); return r; }
DEV float xexp(float a) { float r; asm volatile("v_exp_f32 %0, %1" : "=v"(r) : "v"(a)); return r; }
DEV float xrcp(float a) { float r; asm volatile("v_rcp_f32 %0, %1" : "=v"(r) : "v"(a)); return r; }
DEV u32 xpk(float a, float b) { u32 r; asm volatile("v_cvt_pk_bf16_f32 %0, %1, %2" : "=v"(r) : "v"(a), "v"(b)); return r; }
DEV float xlo(u32 a) { float r; asm volatile("v_lshlrev_b32 %0, 16, %1" : "=v"(r) : "v"(a)); return r; }
DEV float xhi(u32 a) { float r; asm volatile("v_and_b32 %0, 0xffff0000, %1" : "=v"(r) : "v"(a)); return r; }

DEV void dma5(rsrc_t r, u32 vo, u32 so, u32 ld) {
  u32 t;
  asm volatile("s_mov_b32 m0, %4\n\ts_add_u32 %0, %3, 0x1000\n\t"
               "buffer_load_dwordx4 %1, %2, %3 offen lds\n\t"
               "buffer_load_dwordx4 %1, %2, %3 offen offset:1024 lds\n\t"
               "buffer_load_dwordx4 %1, %2, %3 offen offset:2048 lds\n\t"
               "buffer_load_dwordx4 %1, %2, %3 offen offset:3072 lds\n\t"
               "s_add_u32 m0, m0, 0x1000\n\ts_nop 0\n\t"
               "buffer_load_dwordx4 %1, %2, %0 offen lds"
               : "=&s"(t) : "v"(vo), "s"(r), "s"(so), "s"(ld) : "memory", "scc");
}
DEV void dma5kv(rsrc_t r, u32 vo, u32 so, u32 ld) {
  u32 t;
  asm volatile("s_mov_b32 m0, %4\n\ts_add_u32 %0, %3, 0x1000\n\t"
               "buffer_load_dwordx4 %1, %2, %3 offen sc1 lds\n\t"
               "buffer_load_dwordx4 %1, %2, %3 offen offset:1024 sc1 lds\n\t"
               "buffer_load_dwordx4 %1, %2, %3 offen offset:2048 sc1 lds\n\t"
               "buffer_load_dwordx4 %1, %2, %3 offen offset:3072 sc1 lds\n\t"
               "s_add_u32 m0, m0, 0x1080\n\ts_nop 0\n\t"
               "buffer_load_dwordx4 %1, %2, %0 offen sc1 lds"
               : "=&s"(t) : "v"(vo), "s"(r), "s"(so), "s"(ld) : "memory", "scc");
}
template <typename T> DEV T anyv() { T r; asm volatile("" : "=v"(r)); return r; }
DEV void usev(u32x4 x) { asm volatile("" :: "v"(x)); }
DEV void usev2(u32x4 x, u32x4 y) { asm volatile("" :: "v"(x), "v"(y)); }
template <u32 K> DEV u32 vk() { u32 r; asm volatile("v_mov_b32 %0, %1" : "=v"(r) : "i"(K)); return r; }
#define OPQV(x) asm volatile("" : "+v"(x))
#define OPQS(x) asm volatile("" : "+s"(x))

#define SLPX 1
template <int SLP> DEV void spin_ge(u32 a, int thr) {
  u32 t, n;
  asm volatile("s_mov_b32 %1, %5\n0:\n\tds_read_b32 %0, %2\n\ts_sub_u32 %1, %1, 1\n\ts_waitcnt lgkmcnt(0)\n\ts_cbranch_scc1 1f\n\tv_cmp_gt_i32 vcc, %3, %0\n\ts_cbranch_vccz 1f\n\ts_sleep %4\n\ts_branch 0b\n1:"
               : "=&v"(t), "=&s"(n) : "v"(a), "s"(thr), "i"(SLP * SLPX), "i"(SPIN_MAX) : "memory", "scc", "vcc");
}
DEV void chk(int v, u32 a, int thr) { if (__builtin_expect(__builtin_amdgcn_ballot_w64(v < thr) != 0, 0)) spin_ge<0>(a, thr); }

DEV void sig(GAS unsigned char* ws, u32 so, u32 vo) { (void)__hip_atomic_fetch_add((GAS u32*)(ws + so + vo), vk<1>(), RLX_AGENT); }
template <int SLP> DEV void poll_ge(GAS unsigned char* ws, u32 so, u32 tgt) {
  GAS u32* p = (GAS u32*)(ws + so + vk<0>());
#pragma unroll 1
  for (int n = POLL_MAX; n > 0; --n) {
    if (__builtin_amdgcn_readfirstlane(__hip_atomic_load(p, RLX_AGENT)) >= tgt) break;
    if (SLP) __builtin_amdgcn_s_sleep(SLP);
  }
  __builtin_amdgcn_fence(__ATOMIC_ACQUIRE, "wavefront");
  asm volatile("" ::: "memory");
}

#define PDV 4
constexpr int PD = PDV;
constexpr int ALA = 12;

template <int F> struct IC { static constexpr int v = F; };
template <typename T, T... I> struct Seq {};
template <int B, typename Fn, int... I> DEV void sfor_(Fn&& fn, Seq<int, I...>) { const int d[] = {0, (fn(IC<B + I>{}), 0)...}; (void)d; }
template <int B, int E, typename Fn> DEV void sfor(Fn&& fn) { sfor_<B>(fn, __make_integer_seq<Seq, int, E - B>{}); }

struct Ctx { u32 l16, l16h, l16t, mb; };
template <u32 OFF, typename T> DEV T far_ld(const Ctx& c) { return OFF < 65536 ? lds_ld<T>(c.l16 + OFF) : OFF < 131072 ? lds_ld<T>(c.l16h + (OFF - 65536)) : lds_ld<T>(c.l16t + (OFF - 131072)); }
DEV void freed(const Ctx& c) { (void)__builtin_amdgcn_atomic_inc32((LAS u32*)(c.mb + 1024), c.l16h, __ATOMIC_RELAXED, "workgroup"); }

constexpr int K1 = 26;
constexpr int gu_tile(int f) { return f < 3 * K1 ? f % 3 : f < 120 ? (f - 3 * K1) / (40 - K1) : f / 40; }
constexpr int gu_ks(int f) { return f < 3 * K1 ? f / 3 : f < 120 ? K1 + (f - 3 * K1) % (40 - K1) : f % 40; }
constexpr int gu_end(int t) { return t < 3 ? 3 * K1 + (t + 1) * (40 - K1) : 40 * t + 40; }
constexpr int a_at(int f) { for (int k = ALA; k < 40; ++k) if ((k - ALA) * 5 / 2 == f) return k; return -1; }
constexpr int ag_at(int f) { for (int k = ALA; k < 40; ++k) if ((k - ALA) * 9 / 4 == f) return k; return -1; }
#define GP 5
#define WEV 2
#define DRAIN 20
#define CAPD 4
#define CAPG 5
#define GPAUSE 6
#define QPAUSE 79
constexpr int NCK = 13;
constexpr int CK_N[NCK + 1] = {30, 150, 170, 200, 230, 250, 280, 295, 315, 335, 355, 375, 395, 415};
constexpr int ck_thr(int i) { return i == 0 ? 41 : CK_N[i + 1] <= 280 ? 26 + CK_N[i + 1] / 10 : 54 + (CK_N[i + 1] - 280) / 5; }
constexpr int ck_at(int n) { for (int i = 0; i < NCK; ++i) if (CK_N[i] == n) return i; return -1; }
constexpr int EV_CK[5] = {5, 7, 9, 10, 11};
constexpr int EV_TILE[5] = {1, 3, 4, 5, 6};
constexpr int T_EV(int e) { return ck_thr(EV_CK[e]); }
constexpr int hb_time(int b) { const int e = b < 16 ? 0 : b < 32 ? 1 : b / 8 - 2, t0 = CK_N[EV_CK[e]] - GP, i = b < 32 ? b % 16 : b % 8; return t0 + (e == 1 || e == 4 ? i * 3 / 2 : i * 2); }
constexpr int hb_at(int tau) { for (int b = 0; b < 54; ++b) if (hb_time(b) == tau) return b; return -1; }
constexpr int T_RES = 322;
constexpr int cq_at(int f) { return f == 56 ? 0 : f == 64 || f == 112 ? 8 : f == 153 || f == 193 ? 1024 : f == 233 ? 1032 : -1; }
#define NFL 4
constexpr bool fr_at(int n) { return n < 415 && (n % 10 == 9 || (n >= 280 && n % 5 == 4)); }
constexpr int fr_cnt(int n) { return n < 280 ? 29 + (n + 1) / 10 : 57 + (n - 279) / 5; }
struct Plan { int op[7 * 69], first[833], st[7], sig[5], last[7]; };
constexpr int fixed_ops(int tau) { const int n = tau + GP; int c = 1;
  if (tau % WEV == WEV - 1) ++c;
  if (ck_at(n) >= 0) c += 2;
  if (ck_at(n + 1) >= 0) ++c;
  if (ck_at(n + 6) >= 0) ++c;
  if (n % 10 == 9) ++c;
  if (hb_at(tau) >= 0) c += hb_at(tau) % 8 < 2 ? 2 : 1;
  if (cq_at(tau) >= 0) ++c;
  return c; }
constexpr Plan make_plan() { Plan p{}; int o = 0;
  int stage[56] = {}, prod[56] = {}, pk[28] = {}, sto[7] = {};
  int done[56] = {};
  constexpr int CH[7] = {0, 1, 2, 3, 5, 6, 7};
  for (int e = 0; e < 5; ++e) p.sig[e] = -1;
  for (int q = 0; q < 832; ++q) { p.first[q] = o;
    const int tau = q / 2;
    if (tau < 280 && (q & 1)) continue;
    int cap = tau < 280 ? CAPG - fixed_ops(tau) : CAPD;
    for (int e = 0; e < 5; ++e) if (p.sig[e] == tau && !(q & 1)) cap -= 1;
    int stile = -1;
    for (int t = 0; t < 7; ++t) if (!sto[t] && pk[4 * t] && pk[4 * t + 1] && pk[4 * t + 2] && pk[4 * t + 3] && cap >= 2 && (tau < 280 || (q & 1))) { stile = t; cap -= 2; break; }
    for (int i = 0; i < 28; ++i) if (!pk[i] && done[2 * i] && done[2 * i + 1] && cap >= 1 && (i < 4 || sto[i / 4 - 1])) { pk[i] = 1; --cap; p.op[o++] = i / 4 * 128 + 64 + i % 4; }
    int fl = 0;
    for (int i = 0; i < 56; ++i) if (stage[i] > 0 && !pk[i / 2]) ++fl;
    for (int i = 0; i < 56; ++i) { const int t = i / 8;
      if (stage[i] == 7 || tau < gu_end(t) + 1) continue;
      if (stage[i] == 0 && fl >= (tau < 270 ? NFL : 8)) break;
      const int k = CH[stage[i]], c = k == 3 || k == 6 ? 2 : 1;
      const bool hadp = prod[i];
      if (stage[i] >= 3 && !prod[i] && cap >= 1 && (k == 7 || cap != c)) { prod[i] = 1; --cap; p.op[o++] = t * 128 + 32 + i % 8; p.last[t] = tau; }
      if (cap >= c && (k != 7 || hadp)) { if (!stage[i]) ++fl; ++stage[i]; cap -= c; p.op[o++] = t * 128 + k * 8 + i % 8; } }
    for (int i = 0; i < 56; ++i) done[i] = stage[i] == 7;
    if (stile >= 0) { sto[stile] = 1; p.op[o++] = stile * 128 + 72; p.st[stile] = tau; for (int e = 0; e < 5; ++e) if (EV_TILE[e] == stile) { int x = tau + DRAIN; while (!fr_at(x + 1 + GP)) ++x; p.sig[e] = x; } } }
  p.first[832] = o; return p; }
constexpr Plan PL = make_plan();
static_assert(PL.last[0] < 120 && PL.last[1] < 160 && PL.last[2] < 200 && PL.last[3] < 240 && PL.first[832] == 7 * 69, "accumulator reuse");
constexpr int sig_at(int tau) { for (int e = 0; e < 5; ++e) if (PL.sig[e] == tau) return e; return -1; }
constexpr int vm_after(int t0, int t1) {
  int n = 0;
  for (int x = t0 + 1; x < t1; ++x) { for (int t = 0; t < 7; ++t) if (PL.st[t] == x) ++n;
    if (hb_at(x) >= 0) ++n;
    if (x == T_RES) n += 3; }
  return n;
}

constexpr int DG[11] = {0, 45, 90, 140, 210, 250, 279, 299, 349, 379, 414};
constexpr int dg_at(int f) { for (int i = 0; i < 11; ++i) if (DG[i] == f) return i + 1; return -1; }
#define MBW(w) (512 + ((w) & 1) * 256 + ((w) >> 1 & 1) * 128)
#define LANES() int lane; asm volatile("s_nop 2\n\tv_mbcnt_lo_u32_b32 %0, -1, 0\n\tv_mbcnt_hi_u32_b32 %0, -1, %0" : "=v"(lane)); const int l31 = lane & 31, hh = lane >> 5, l15 = lane & 15, g = lane >> 4; (void)l31; (void)hh; (void)l15; (void)g; \
  u32 pk_ = pk; OPQS(pk_); const u32 rb = pk_ & 15, s = (pk_ >> 4) & 15, wq = (pk_ >> 8) & 3, w = (pk_ >> 8) & 7, hd = w >> 2, layer = pk_ >> 12, cs = s >> 1, half = s & 1; \
  Ctx c; c.l16 = lane * 16; c.l16h = lane * 16 + 65536; c.l16t = lane * 16 + 131072; c.mb = L_FULL + (-hh & (MBW(wq) - 128)) + (lane & 16 & (hh - 1)) * 16 + lane * 4; OPQV(c.l16h); OPQV(c.l16t); OPQV(c.mb); \
  const u32 gfl = OFF_FLAG + rb * 131072 + layer * 2048, fl = gfl + s * 8192; \
  const int fb = layer * LS, eb = layer * LF; \
  const u32 G = rb * 8 + half * 4 + wq, xe_s = OFF_X + (G >> 1) * XT + 5 * cs * 1024 + (G & 1) * 256, kvb = OFF_KV + layer * KVSZ + s * 64 * KVT; \
  (void)fl; (void)hd; (void)xe_s; (void)kvb; (void)fb; (void)eb;

#define STH(i) ST(i)
#define RFL(x) __builtin_amdgcn_readfirstlane(x)
struct Str { rsrc_t rs; u32 l16, fa, fl, seq, hl; };
template <int K> DEV void stream(Str& t, u32 so, u32 lu, int n, int thr, int dthr) {
  u32 ld = lu * UN + t.hl, fr, x, tries, seq = t.seq, m;
  so += t.hl; t.seq += 4 * n;
  so = RFL(so); ld = RFL(ld); thr = RFL(thr); seq = RFL(seq);
  asm volatile("s_mov_b32 %[n], %[n0]\n\ts_mov_b32 %[tr], %[t0]\n\ts_branch 2f\n1:\n\ts_sleep 1\n\ts_sub_u32 %[tr], %[tr], 1\n\ts_cmp_lt_i32 %[tr], 0\n\ts_cbranch_scc1 3f\n2:\n\tds_read_b32 %[fr], %[fa]\n\ts_waitcnt lgkmcnt(0)\n"
               "0:\n\tv_cmp_gt_i32 vcc, %[thr], %[fr]\n\ts_mov_b32 m0, %[ld]\n\ts_add_u32 %[x], %[so], 0x1000\n\ts_cbranch_vccnz 1b\n"
               "3:\n\tbuffer_load_dwordx4 %[la], %[r], %[so] offen lds\n\t"
               "buffer_load_dwordx4 %[la], %[r], %[so] offen offset:1024 lds\n\t"
               "buffer_load_dwordx4 %[la], %[r], %[so] offen offset:2048 lds\n\t"
               "buffer_load_dwordx4 %[la], %[r], %[so] offen offset:3072 lds\n\t"
               "s_add_u32 m0, m0, 0x1000\n\ts_add_u32 %[so], %[so], %[un]\n\t"
               "buffer_load_dwordx4 %[la], %[r], %[x] offen lds\n\t"
               "s_mov_b32 m0, %[fl]\n\ts_add_u32 %[seq], %[seq], 4\n\t"
               "buffer_load_dword off, %[r], %[seq] lds\n\t"
               "s_add_u32 %[ld], %[ld], %[un]\n\ts_add_u32 %[thr], %[thr], %[dt]\n\t"
               "s_cmp_ge_u32 %[ld], %[lim]\n\ts_cselect_b32 %[x], %[lim], 0\n\ts_sub_u32 %[ld], %[ld], %[x]\n\t"
               "s_sub_u32 %[n], %[n], 1\n\ts_waitcnt vmcnt(%[k])\n\ts_cbranch_scc0 0b"
               : [fr] "=&v"(fr), [x] "=&s"(x), [so] "+s"(so), [ld] "+s"(ld), [thr] "+s"(thr), [seq] "+s"(seq), [n] "=&s"(m), [tr] "=&s"(tries)
               : [fa] "v"(t.fa), [fl] "s"(RFL(t.fl)), [la] "v"(t.l16), [r] "s"(t.rs), [dt] "i"(dthr), [lim] "i"(15 * UN), [un] "i"(UN), [k] "i"(K), [n0] "i"(n - 1), [t0] "i"(SPIN_MAX) : "memory", "scc", "vcc");
}
#define RGU 0
#define RG1 0
#define RG2 0
#define NR 10
#define HLS 128
#define PDLY 2
#define PSLP 2
#define CPRIO 2
#define HPRIO 0
#define NA 30
#define NB 24
#define NC 12

__global__ void __launch_bounds__(512, 2) mega(unsigned short* __restrict__ out, unsigned char* __restrict__ wsp) {
  GAS unsigned char* ws = (GAS unsigned char*)wsp;
  const int w0 = __builtin_amdgcn_readfirstlane(threadIdx.x >> 6);
  const int bx = blockIdx.x & 7, br = blockIdx.x >> 3;
  const bool isC = w0 < 4;
  u32 pk = ((bx & 3) * 4 + (br & 3)) | (((bx >> 2) * 8 + (br >> 2)) << 4) | (w0 << 8);
  const rsrc_t rs = __builtin_amdgcn_make_buffer_rsrc((void*)wsp, 0, OFF_END, 0x00020000);
#define XE_A ((g >> 1) * 1024 + (l15 + 32 * (g & 1)) * 16)
#define XE_C (4096 + (l15 + 32 * (g >> 1)) * 16)
#define STR_INIT(first) Str t; t.rs = rs; t.l16 = c.l16; t.fa = L_FREE + (lane & 4) * 256 + MBW(lane) + (lane & 7) * 4; t.fl = L_FULL + wq * 256; t.seq = OFF_SEQ + (fb + (first)) * 4; t.hl = wq * 5120;
  {
    LANES();
    lds_post(c.mb + (isC ? 1024 : 2048), isC ? 0 : 16);
    if (!isC) { lds_post(c.mb, g < 2 ? 0 : g == 2 ? T_CS - 1 : T_KV - 1); lds_post(L_ATT + lane * 4, 0); }
    BAR();
    if (w == 4 || w == 5) { STR_INIT(0); stream<NA>(t, OFF_WQ + s * 163840, 0, 15, -100, 0); }
    if (w >= 6) {
      const u32 so = OFF_CS + s * 7680;
      for (int q = w - 6; q < (w == 6 ? 1 : 3); ++q) {
        u32x4 d;
        for (int j = 0; j < 4; ++j) { float v = 0;
          for (int i = 0; i < 5; ++i) v += asf(__builtin_amdgcn_raw_buffer_load_b32(rs, ((q * 4 + j) * 32 + l31) * 4 + i * 1536, so, 0));
          const u32 p = split2(v), x = hh ? p >> 16 : p & 0xffffu; d[j] = (q * 4 + j < 11) ? x | (x << 16) : 0u; }
        lds_st<u32x4>(L_CS + q * 1024 + c.l16, d);
      }
      if (w == 6) lds_post(L_FULL + MBW(g) + l15 * 4, T_EV(0) - 1);
    }
  }

#pragma unroll 1
  for (; pk < 8192; pk += 4096) {
    u32x4 qm; u32x2 qr;
    ST(0);
    if (isC) {
      LANES();
      __builtin_amdgcn_s_setprio(CPRIO);
      if (layer) poll_ge<0>(ws, fl + (F_X + wq) * 128, 16);
      STQ(0);
      const u32 xso = OFF_X + layer * 2 * XSZ + (rb * 4 + wq) * XT;
      u32x4 A[40], W[160];
      sfor<0, ALA>([&](auto i) { constexpr int k = decltype(i)::v; A[k] = ld16(rs, c.l16, xso + k * 1024); });
      u32x2 sp[4];
      for (int i = 0; i < 4; ++i) sp[i] = ld8(rs, hh * 1024 + l31 * 8 + i * 256, xso + 40960);
      spin_ge<0>(c.mb, fb + 1);
      f32x16 acc[3], aq;
      const f32x16 z16 = {};
      float rstd; u32 mo; int fv; u32x4 cq;
      auto ahead = [&](auto ni) { constexpr int n = decltype(ni)::v;
        if constexpr (n < 160) {
          if constexpr (n % 10 == 0 && n) chk(fv, c.mb, fb + n / 10 + 1);
          W[n] = far_ld<(n % 150) * 1024, u32x4>(c);
          if constexpr (n % 10 == 9) freed(c);
          if constexpr (n % 10 == 5 && n < 150) fv = lds_word(c.mb);
        } };
      sfor<0, PD>(ahead);
      PIN();
      u32 kp[22];
      const u32 kso = kvb + (rb * 4 + wq) * KVT;
      const float qsc = 0.22360679774997896f * 1.4426950408889634f;
      sfor<0, 160>([&](auto fi) {
        constexpr int f = decltype(fi)::v, t = f % 3, ks = f < 120 ? f / 3 : f - 120, n = f + PD;
        if constexpr (f >= 120) aq = mm32p(W[f], A[ks], aq);
        else if constexpr (ks == 0) acc[t] = mm32p(W[f], A[ks], z16); else acc[t] = mm32p(W[f], A[ks], acc[t]);
        ahead(IC<n>{});
        if constexpr (a_at(f) >= 0) { constexpr int k = a_at(f); A[k] = ld16(rs, c.l16, xso + k * 1024); }
        if constexpr (f == 40) {
          float sm = asf(sp[0][0]) + asf(sp[1][0]) + asf(sp[2][0]) + asf(sp[3][0]), sq = asf(sp[0][1]) + asf(sp[1][1]) + asf(sp[2][1]) + asf(sp[3][1]);
          sm = swap32_add(sm); sq = swap32_add(sq);
          const float mean = sm * (1.f / 640.f);
          rstd = __builtin_amdgcn_rsqf(fmaxf(sq * (1.f / 640.f) - mean * mean, 0.f) + 1e-5f);
          mo = split2(-mean);
          OPQV(rstd); OPQV(mo);
        }
        if constexpr (f == 90) cq = far_ld<L_CS, u32x4>(c);
        if constexpr (f == 98) acc[0] = mm32k8p(u32x2{cq[0], cq[1]}, u32x2{mo, 0u}, acc[0]);
        if constexpr (f == 102) acc[1] = mm32k8p(u32x2{cq[0], cq[1]}, u32x2{0u, mo}, acc[1]);
        if constexpr (f == 106) acc[2] = mm32k8p(u32x2{cq[2], cq[3]}, u32x2{mo, 0u}, acc[2]);
        if constexpr (f == 119) aq = mm32k8p(u32x2{cq[2], cq[3]}, u32x2{0u, mo}, z16);
        if constexpr (f >= 121 && f < 132) {
          constexpr int j = (f - 121) * 2, tt = j / 8, r = (j % 8) * 2;
          if constexpr (j == 18) { const float r2 = rstd * qsc; kp[j] = mulpk(acc[2][4], acc[2][5], r2); kp[j + 1] = mulpk(acc[2][6], acc[2][7], r2); }
          else { kp[j] = mulpk(acc[tt][r], acc[tt][r + 1], rstd); kp[j + 1] = mulpk(acc[tt][r + 2], acc[tt][r + 3], rstd); }
          if constexpr (j < 16 && (j & 2)) {
            constexpr int q = j / 4;
            const u32 vo = q < 2 ? c.l16 : (2 * l31 + hh) * 16;
            const u32x4 v = {kp[j - 2], kp[j - 1], kp[j], kp[j + 1]};
            st16(v, rs, vo, kso + q * 1024);
            lds_st<u32x4>(L_KV + (8 + wq) * KVL + q * 1024 + vo, v);
          }
          if constexpr (j == 20) {
            const u32 vo = (2 * l31 + hh) * 16;
            const u32x4 v = {kp[16], kp[17], kp[20], kp[21]};
            st16(v, rs, vo, kso + 4096);
            lds_st<u32x4>(L_KV + (8 + wq) * KVL + 4224 + vo, v);
            lds_st<u32x2>(L_QH + wq * 1536 + 1024 + lane * 8, u32x2{kp[18], kp[19]});
          }
        }
        if constexpr (f == 0) STQ(1);
        if constexpr (f == 60) STQ(2);
        if constexpr (f == 120) STQ(3);
        PIN();
        if constexpr (f == 152) {
          WAITV(0);
          if (c.l16 < 32 && c.l16 < (15 - rb) * 16) sig(ws, fl + F_KV * 128 + 131072, c.l16 * 8192);
          PIN();
        }
      });
      STQ(4);
      {
        const float r2 = rstd * qsc;
        lds_st<u32x4>(L_QH + wq * 1536 + c.l16, u32x4{pk2(aq[8] * r2, aq[9] * r2), pk2(aq[10] * r2, aq[11] * r2), pk2(aq[12] * r2, aq[13] * r2), pk2(aq[14] * r2, aq[15] * r2)});
        qm = u32x4{pk2(aq[0] * r2, aq[1] * r2), pk2(aq[2] * r2, aq[3] * r2), pk2(aq[4] * r2, aq[5] * r2), pk2(aq[6] * r2, aq[7] * r2)};
        qr = u32x2{kp[18], kp[19]};
      }
      __builtin_amdgcn_s_setprio(0);
    } else {
      LANES();
      if (wq < 2) {
        STR_INIT(15);
        stream<57>(t, OFF_WQ + s * 163840 + 15 * UN, 0, 1, eb + 1, 0);
        stream<57>(t, OFF_WO + cs * 102400, 8, 8, eb + 9, 1);
      } else if (wq == 3) {
        spin_ge<2>(L_FREE + MBW(lane) + (lane & 3) * 4, eb + 8);
        const u32 one = ((int)(7 - rb * 4 - lane) >> 31) & 0x3f803f80u;
        if (lane < 12) lds_st<u32x2>(L_KV + lane * KVL + 4096, u32x2{one, one});
        lds_post(L_FULL + MBW(g) + 64 + l15 * 4, fb + T_X2 - 1);
      }
      asm volatile("" : "=v"(qm), "=v"(qr));
    }
    {
      LANES();
      const u32 qt = hd ? 3 - wq : wq;
      f32x16 cm;
      { u32 b = (u32)(l31 - 4 * hh) << 18; OPQV(b);
        for (int r = 0; r < 16; ++r) cm[r] = asf((b - ((8 * (r >> 2) + (r & 3)) << 18)) & 0xff800000u); }
      const u32 tb = L_KV + qt * KVL;
      u32 aK = tb + hd * 1024 + c.l16;
      u32 aR = hh ? tb + 4096 : tb + 4224 + l31 * 32 + hd * 16;
      u32 aV[4];
      { const int q = l15 >> 2, p = l15 & 3;
        for (int r = 0; r < 4; ++r) { const int key = 8 * r + 4 * hh + q;
          aV[r] = !(g & 1) ? tb + 2048 + hd * 1024 + key * 32 + (p & 1) * 16 + (p >> 1) * 8 : p == 0 ? tb + 4224 + key * 32 + hd * 16 + 8 : tb + 4096; OPQV(aV[r]); } }
      OPQV(aK); OPQV(aR); OPQV(cm);
      u32 aA = L_ATT + lane * 4; OPQV(aA);
      BAR();
      if (!isC) { qm = lds_ld<u32x4>(L_QH + qt * 1536 + c.l16); qr = lds_ld<u32x2>(L_QH + qt * 1536 + 1024 + (32 + l31) * 8); }
      STQ(5);
      u32x4 Km[9]; u32x2 Kr[9], Vt[9][4]; int av[9];
      auto ldk = [&](auto ii) { constexpr int i = decltype(ii)::v; Km[i] = lds_ld<u32x4>(aK + (8 - i) * KVL); Kr[i] = lds_ld<u32x2>(aR + (8 - i) * KVL); };
      auto ldv = [&](auto ii) { constexpr int i = decltype(ii)::v; for (int r = 0; r < 4; ++r) Vt[i][r] = lds_tr(aV[r] + (8 - i) * KVL); };
      ldk(IC<0>{}); av[1] = lds_word(aA); ldk(IC<1>{}); ldv(IC<0>{});
      const f32x16 z16 = {};
      f32x16 sc[9], o;
      if (hh) qr = u32x2{0u, 0u};
      sc[0] = mm32k8(Kr[0], qr, mm32(Km[0], qm, cm));
      {
        float m = fmaxf(sc[0][0], sc[0][1]);
        for (int r = 2; r < 16; r += 2) m = fmaxf(fmaxf(m, sc[0][r]), sc[0][r + 1]);
        m = swap32_max(m);
        const u32 sh = hh ? split2(-m) : 0u;
        sc[0] = mm32k8p(Kr[0], u32x2{sh, 0u}, sc[0]);
        qr[0] |= sh;
      }
      sfor<0, 9>([&](auto ii) {
        constexpr int i = decltype(ii)::v;
        if constexpr (i >= 1) {
          const int need = i <= (int)qt ? 0 : layer * 2 + (i <= (int)qt + 4 ? 1 : 2);
          if (__builtin_expect(__builtin_amdgcn_ballot_w64(av[i] < need) != 0, 0)) {
            spin_ge<1>(aA, need);
            ldk(ii); ldv(ii);
            if constexpr (i < 8) { av[i + 1] = lds_word(aA); ldk(IC<i + 1>{}); }
            if constexpr (i < 8) sc[i] = mm32k8(Kr[i], qr, mm32(Km[i], qm, z16)); else sc[i] = mm32k8(Kr[i], qr, mm32(Km[i], qm, cm));
            __builtin_amdgcn_s_waitcnt(0xC07F);
          }
        }
        if constexpr (i < 7) sc[i + 1] = mm32k8p(Kr[i + 1], qr, mm32(Km[i + 1], qm, z16));
        if constexpr (i == 7) sc[8] = mm32k8p(Kr[8], qr, mm32(Km[8], qm, cm));
        if constexpr (i < 8) ldv(IC<i + 1>{});
        if constexpr (i < 7) { av[i + 2] = lds_word(aA); ldk(IC<i + 2>{}); }
        PIN();
        float e[16];
        for (int r = 0; r < 8; ++r) e[r] = __builtin_amdgcn_exp2f(sc[i][r]);
        const u32x4 p0 = {pk2(e[0], e[1]), pk2(e[2], e[3]), pk2(e[4], e[5]), pk2(e[6], e[7])};
        const u32x4 v0 = {Vt[i][0][0], Vt[i][0][1], Vt[i][1][0], Vt[i][1][1]}, v1 = {Vt[i][2][0], Vt[i][2][1], Vt[i][3][0], Vt[i][3][1]};
        if constexpr (i == 0) o = mm32p(v0, p0, z16); else o = mm32p(v0, p0, o);
        for (int r = 8; r < 16; ++r) e[r] = __builtin_amdgcn_exp2f(sc[i][r]);
        const u32x4 p1 = {pk2(e[8], e[9]), pk2(e[10], e[11]), pk2(e[12], e[13]), pk2(e[14], e[15])};
        o = mm32p(v1, p1, o);
        PIN();
        if constexpr (i == 4) if (!isC) STQ(12);
        if constexpr (i == 0) {
          for (int r = 0; r < 16; ++r) cm[r] = asf(asu(cm[r]) ^ 0xff800000u);
          OPQV(cm);
          if (!isC) STQ(11);
          if (w >= 6) {
            poll_ge<0>(ws, fl + F_KV * 128, rb >= 2 ? 8 : rb * 4);
            STQ(8);
            const u32 j = w == 6 ? 6 : 4, src = kvb + (rb * 4 - 8 + j) * KVT;
            dma5kv(rs, c.l16, rb >= 1 ? src + KVT : OFF_ZERO, L_KV + (j + 1) * KVL);
            dma5kv(rs, c.l16, rb >= 1 ? src : OFF_ZERO, L_KV + j * KVL);
            dma5kv(rs, c.l16, rb >= 2 ? src - 3 * KVT : OFF_ZERO, L_KV + (j - 3) * KVL);
            dma5kv(rs, c.l16, rb >= 2 ? src - 4 * KVT : OFF_ZERO, L_KV + (j - 4) * KVL);
            const u32 pa = (aA & ~128u) + (w & 1) * 128;
            WAITV(10); lds_post(pa, layer * 2 + 1);
            STQ(9);
            WAITV(0); lds_post(pa, layer * 2 + 2);
            STQ(10);
          }
          PIN();
        }
      });
      if (__builtin_amdgcn_ballot_w64(!(o[12] < 1.0e12f && o[12] > 1.0e-12f)) != 0) {
        const int gt = rb * 4 + qt, nv = gt + 1 < 9 ? gt + 1 : 9;
        if (hh) qr[0] = 0u;
        float m = -3.0e38f;
        for (int pass = 0; pass < 2; ++pass) {
          o = z16;
#pragma unroll 1
          for (int i = 0; i < nv; ++i) {
            const u32 d = (8 - i) * KVL;
            const u32 ma = (i & 7) ? 0u : ~0u, mb = i ? 0u : 0xff800000u;
            f32x16 x;
            for (int r = 0; r < 16; ++r) x[r] = asf((asu(cm[r]) & ma) ^ mb);
            x = mm32k8(lds_ld<u32x2>(aR + d), qr, mm32(lds_ld<u32x4>(aK + d), qm, x));
            if (pass == 0) { for (int r = 0; r < 16; ++r) m = fmaxf(m, x[r]); }
            else {
              for (int r = 0; r < 16; ++r) x[r] = __builtin_amdgcn_exp2f(x[r] - m);
              const u32x4 p0 = {pk2(x[0], x[1]), pk2(x[2], x[3]), pk2(x[4], x[5]), pk2(x[6], x[7])};
              const u32x4 p1 = {pk2(x[8], x[9]), pk2(x[10], x[11]), pk2(x[12], x[13]), pk2(x[14], x[15])};
              const u32x2 a = lds_tr(aV[0] + d), b = lds_tr(aV[1] + d), e = lds_tr(aV[2] + d), f = lds_tr(aV[3] + d);
              o = mm32(u32x4{a[0], a[1], b[0], b[1]}, p0, o); o = mm32(u32x4{e[0], e[1], f[0], f[1]}, p1, o);
            }
          }
          m = swap32_max(m);
        }
      }
      (void)__hip_atomic_fetch_add((LAS u32*)(c.mb + 1024 + hd * 1024), 1 + hd * (LF - 1), RLX_WG);
      STQ(6);
      const float il = __builtin_amdgcn_rcpf(o[12]);
      const u32x4 om = {pk2(o[0] * il, o[1] * il), pk2(o[2] * il, o[3] * il), pk2(o[4] * il, o[5] * il), pk2(o[6] * il, o[7] * il)};
      const u32 r0 = pk2(o[8] * il, o[9] * il), r1 = pk2(o[10] * il, o[11] * il);
      const u32x4 orr = {r0, r1, (u32)__builtin_amdgcn_mov_dpp(r0, 0xf5, 0xf, 0xf, true), (u32)__builtin_amdgcn_mov_dpp(r1, 0xf5, 0xf, 0xf, true)};
      const u32 oso = OFF_O + layer * OSZ + (rb * 8 + qt * 2) * 20480 + (s >> 3) * 10240;
      const u32 tg = (l31 >> 4) * 20480;
      st16(om, rs, tg + (s & 7) * 1024 + (l15 + 16 * (2 * hd + hh)) * 16, oso);
      if (!(lane & 33)) st16(orr, rs, tg + 8192 + ((s & 7) * 2 + hd) * 128 + l15 * 8, oso);
      WAITV(0);
      if (lane < 8) sig(ws, gfl + (qt >> 1) * 8192 + (F_O + (s >> 3) + (qt & 1) * 2) * 128, lane * 16384);
      STQ(7);
      if (isC) { STF(0, 1); STF(1, 2); STF(2, 3); STF(3, 24); STF(4, 4); } else { STF(11, 11); STF(12, 12); if (w >= 6) { STF(8, 8); STF(9, 9); STF(10, 10); } }
      STF(5, 5); STF(6, 6); STF(7, 7);
    }
    if (isC) {
      u32 res[10];
      auto ldres = [&](u32 xs, int l15, int g) {
        u32x4 a = ld16(rs, XE_A, xs), b = ld16(rs, XE_A + 2048, xs); u32x2 d = ld8(rs, XE_C + (g & 1) * 8, xs);
        res[0] = a[0]; res[1] = a[1]; res[2] = a[2]; res[3] = a[3]; res[4] = b[0]; res[5] = b[1]; res[6] = b[2]; res[7] = b[3]; res[8] = d[0]; res[9] = d[1]; };
      auto epi = [&](f32x4 (&acc)[2][5], int which) {
        LANES();
        const u32 xb = (which ? L_X2 : L_X1) + c.l16;
        for (int j = 0; j < 5; ++j) lds_st<f32x4>(xb + (wq ^ 1) * 5120 + j * 1024, acc[1][j]);
        freed(c);
        if (which) for (int j = 0; j < 5; ++j) { acc[0][j] += f32x4{bflo(res[2 * j]), bfhi(res[2 * j]), bflo(res[2 * j + 1]), bfhi(res[2 * j + 1])}; OPQV(acc[0][j]); }
        spin_ge<0>(L_FREE + MBW(wq ^ 1) + l31 * 4, eb + (which ? 85 : 28));
        float v[20];
        for (int j = 0; j < 5; ++j) { f32x4 p = lds_ld<f32x4>(xb + wq * 5120 + j * 1024);
          for (int i = 0; i < 4; ++i) v[4 * j + i] = acc[0][j][i] + p[i]; }
        freed(c);
        if (!which) STQ(3);
        for (int j = 0; j < 10; ++j) res[j] = pk2(v[2 * j], v[2 * j + 1]);
        if (which == 1 && layer == 1) {
          GAS unsigned char* o = (GAS unsigned char*)out + (size_t)(G * 16 + l15) * 1280 + cs * 160;
          *(GAS u32x4*)(o + g * 16) = u32x4{res[0], res[1], res[2], res[3]};
          *(GAS u32x4*)(o + 64 + g * 16) = u32x4{res[4], res[5], res[6], res[7]};
          *(GAS u32x2*)(o + 128 + g * 8) = u32x2{res[8], res[9]};
        } else {
          const u32 xs = xe_s + (layer * 2 + 1 + which) * XSZ;
          st16(u32x4{res[0], res[1], res[2], res[3]}, rs, XE_A, xs);
          st16(u32x4{res[4], res[5], res[6], res[7]}, rs, XE_A + 2048, xs);
          float sm = 0, sq = 0;
          for (int j = 0; j < 10; ++j) { sm = dot2(res[j], 0x3f803f80u, sm); sq = dot2(res[j], res[j], sq); }
          sm = swap32_add(swap16_add(sm)); sq = swap32_add(swap16_add(sq));
          auto a = __builtin_amdgcn_permlane16_swap(res[8], asu(sm), false, false);
          auto b = __builtin_amdgcn_permlane16_swap(res[9], asu(sq), false, false);
          const u32x4 t = {a[0], b[0], (u32)__builtin_amdgcn_update_dpp(a[1], a[1], 0xf5, 0x2, 0xf, false), (u32)__builtin_amdgcn_update_dpp(b[1], b[1], 0xf5, 0x2, 0xf, false)};
          const u32 vo = g == 1 ? 40960 - cs * 4864 - (G & 1) * 128 + l15 * 8 : XE_C;
          if (!(g & 1) || (g == 1 && !(lane & 1))) st16(t, rs, vo, xs);
          if (!which) STQ(4);
          WAITV(0);
          if (lane < 16) sig(ws, gfl + ((which ? 16 + F_X : F_X2) + half * 2 + (wq >> 1)) * 128, lane * 8192);
        }
      };
      {
        LANES();
        __builtin_amdgcn_s_setprio(CPRIO);
        const u32 kh = wq & 1, th = wq >> 1;
        ldres(xe_s + layer * 2 * XSZ, l15, g);
        Ctx k = c; k.l16 += kh * 5120; k.l16h += kh * 5120; k.l16t += kh * 5120;
        u32x4 B[10][2], W[50];
        int fv;
        auto wld = [&](auto ni) { constexpr int n = decltype(ni)::v;
          if constexpr (n < 50) {
            if constexpr (n == 40) chk(fv, c.mb, fb + 26);
            W[n] = far_ld<((8 + n / 5) % 15) * UN + (n % 5) * 1024, u32x4>(k);
            if constexpr (n % 5 == 4) freed(c);
            if constexpr (n == 35) fv = lds_word(c.mb);
          } };
        f32x4 acc[2][5]; const f32x4 z4 = {};
        for (int j = 0; j < 5; ++j) { acc[0][j] = f32x4{bflo(res[2 * j]), bfhi(res[2 * j]), bflo(res[2 * j + 1]), bfhi(res[2 * j + 1])}; OPQV(acc[0][j]); }
        poll_ge<0>(ws, fl + (F_O + wq) * 128, 16);
        STQ(0);
        const u32 so = OFF_O + layer * OSZ + (rb * 8 + half * 4 + th * 2) * 20480 + kh * 10240;
        const u32 se[2] = {so + kh * 20480, so + (kh ^ 1) * 20480};
        u32 vr = g * 256 + l15 * 8; OPQV(vr);
        auto bld = [&](auto bi) { constexpr int b = decltype(bi)::v, st = b / 2, e = b % 2;
          if constexpr (st < 8) B[st][e] = ld16(rs, c.l16, se[e] + st * 1024);
          else { const u32x2 x = ld8(rs, vr, se[e] + st * 1024), y = ld8(rs, vr, se[e] + st * 1024 + 128); B[st][e] = u32x4{x[0], x[1], y[0], y[1]}; } };
        sfor<0, ALA>(bld);
        spin_ge<0>(c.mb, fb + 24);
        sfor<0, PD>(wld);
        PIN();
        sfor<0, 50>([&](auto fi) {
          constexpr int f = decltype(fi)::v, st = f / 5, jn = f % 5, n = f + PD;
          acc[0][jn] = mm16p(W[f], B[st][0], acc[0][jn]);
          wld(IC<n>{});
          PIN();
          acc[1][jn] = mm16p(W[f], B[st][1], st ? acc[1][jn] : z4);
          if constexpr (f % 2 == 0 && ALA + f / 2 < 20) bld(IC<ALA + f / 2>{});
          if constexpr (f == 0) STQ(1);
          PIN();
        });
        STQ(2);
        epi(acc, 0);
        STQ(5);
        STF(0, 8); STF(1, 27); STF(2, 9); STF(3, 25); STF(4, 26); STF(5, 10);
      }
      {
        LANES();
        const u32 kh = wq & 1, th = wq >> 1;
        u32 xso = OFF_X + (layer * 2 + 1) * XSZ + (rb * 4 + wq) * XT;
        u32 hso = OFF_H + layer * HSZ + (rb * 8 + wq * 2) * HG + (s >> 3) * 27648 + ((s >> 1) & 3) * 1024, hs6 = (s & 7) < 6 ? hso + 24576 : OFF_DUMP;
        u32 hvo = (l31 >> 4) * HG + (l15 + 16 * (2 * (s & 1) + hh)) * 16;
        const u32 bso = OFF_H + layer * HSZ + (rb * 8 + half * 4 + th * 2) * HG + kh * 27648;
        u32 bs0 = bso + kh * HG, bs1 = bso + (kh ^ 1) * HG, k5 = kh * 5120, rso = xe_s + (layer * 2 + 1) * XSZ;
        OPQV(c.l16); OPQV(hvo); OPQS(xso); OPQS(hso); OPQS(hs6); OPQS(bs0); OPQS(bs1); OPQS(k5); OPQS(rso);
        poll_ge<0>(ws, fl + (F_X2 + wq) * 128, 16);
        STQ(0);
        u32x4 A[40], W[415], B[27][2];
        auto ald = [&](auto i) { constexpr int k = decltype(i)::v; A[k] = ld16(rs, c.l16 + (k & 3) * 1024, xso + (k >> 2) * 4096); };
        sfor<0, ALA>(ald);
        u32x2 sp[4];
        for (int i = 0; i < 4; ++i) sp[i] = ld8(rs, hh * 1024 + l31 * 8 + i * 256, xso + 40960);
        spin_ge<0>(c.mb, fb + 29);
        f32x16 acc[7];
        f32x4 ad[2][5];
        const f32x16 z16 = {}; const f32x4 z4 = {};
        sfor<0, GP>([&](auto i) { constexpr int f = decltype(i)::v; W[f] = far_ld<(30 + f) * 1024, u32x4>(c); });
        PIN();
        float k1, k2, vv, ge[8], gp[8]; u32 mo, hp[4]; int fv; u32x2 cq;
        Ctx k;
        auto gop = [&](auto oi) { constexpr int o = PL.op[decltype(oi)::v], t = o / 128, kind = o % 128 / 8, j = o % 8;
          if constexpr (kind == 0) ge[j] = xmul(acc[t][j], acc[t][j]);
          if constexpr (kind == 1) ge[j] = xfma(k2, ge[j], k1);
          if constexpr (kind == 2) ge[j] = xmul(acc[t][j], ge[j]);
          if constexpr (kind == 3) ge[j] = xexp(ge[j]);
          if constexpr (kind == 4) gp[j] = xmul(acc[t][j], acc[t][8 + j]);
          if constexpr (kind == 5) ge[j] = xfma(ge[j], vv, vv);
          if constexpr (kind == 6) ge[j] = xrcp(ge[j]);
          if constexpr (kind == 7) gp[j] = xmul(gp[j], ge[j]);
          if constexpr (kind == 8) hp[j] = xpk(gp[2 * j], gp[2 * j + 1]);
          if constexpr (kind == 9) st16(u32x4{hp[0], hp[1], hp[2], hp[3]}, rs, hvo, t < 6 ? hso + t * 4096 : hs6); };
        auto side = [&](auto ti) {
          constexpr int tau = decltype(ti)::v;
          if constexpr (sig_at(tau) >= 0) WAITV(vm_after(PL.st[EV_TILE[sig_at(tau)]], tau));
          if constexpr (hb_at(tau) >= 0) { constexpr int b = hb_at(tau), st = b / 2, e = b % 2;
            B[st][e] = ld16(rs, c.l16 + (st & 3) * 1024, (e ? bs1 : bs0) + (st >> 2) * 4096); }
          if constexpr (tau == T_RES) ldres(rso, (c.l16 >> 4) & 15, c.l16 >> 8);
        };
        auto ahead = [&](auto ti) { constexpr int tau = decltype(ti)::v, n = tau + GP;
          if constexpr (ck_at(n) >= 0) chk(fv, c.mb, fb + ck_thr(ck_at(n)));
          if constexpr (n < 280) W[n] = far_ld<((30 + n) % 150) * 1024, u32x4>(c);
          else if constexpr (n < 415) W[n] = far_ld<((1 + (n - 280) / 5) % 15) * UN + (n % 5) * 1024, u32x4>(k);
          if constexpr (fr_at(n)) freed(c);
          if constexpr (ck_at(n + 6) >= 0) fv = lds_word(c.mb); };
        auto both = [&](auto ti) { constexpr int tau = decltype(ti)::v;
          constexpr bool w = tau % WEV == WEV - 1 && tau + WEV < 415, bb = tau >= 279 && tau < 414 && tau % 5 == 4;
          if constexpr (w && bb) usev2(W[tau + WEV], B[(tau - 279) / 5][1]); else if constexpr (w) usev(W[tau + WEV]); else if constexpr (bb) usev(B[(tau - 279) / 5][1]); };
        sfor<0, 280>([&](auto fi) {
          constexpr int f = decltype(fi)::v, t = gu_tile(f), ks = gu_ks(f);
          if constexpr (ks == 0 && t >= 3) acc[t] = mm32k8p(cq, (t & 1) ? u32x2{0u, mo} : u32x2{mo, 0u}, z16);
          if constexpr (ks == 0 && t < 3) acc[t] = mm32p(W[f], A[ks], z16); else acc[t] = mm32p(W[f], A[ks], acc[t]);
          if constexpr (f == 238) { k = c; k.l16 += k5; k.l16h += k5; k.l16t += k5; OPQV(k.l16); OPQV(k.l16h); OPQV(k.l16t); }
          ahead(fi);
          if constexpr (ag_at(f) >= 0) ald(IC<ag_at(f)>{});
          if constexpr (f == 40) {
            float sm = asf(sp[0][0]) + asf(sp[1][0]) + asf(sp[2][0]) + asf(sp[3][0]), sq = asf(sp[0][1]) + asf(sp[1][1]) + asf(sp[2][1]) + asf(sp[3][1]);
            sm = swap32_add(sm); sq = swap32_add(sq);
            const float mean = sm * (1.f / 640.f);
            vv = fmaxf(sq * (1.f / 640.f) - mean * mean, 0.f) + 1e-5f;
            const float rstd = __builtin_amdgcn_rsqf(vv);
            k1 = -2.f * 0.7978845608028654f * 1.4426950408889634f * rstd;
            k2 = k1 * 0.044715f * rstd * rstd;
            mo = split2(-mean);
            OPQV(k1); OPQV(k2); OPQV(vv); OPQV(mo);
          }
          if constexpr (f == 62) acc[0] = mm32k8p(cq, u32x2{mo, 0u}, acc[0]);
          if constexpr (f == 63) acc[1] = mm32k8p(cq, u32x2{0u, mo}, acc[1]);
          if constexpr (f == 70) acc[2] = mm32k8p(cq, u32x2{mo, 0u}, acc[2]);
          if constexpr (cq_at(f) >= 0) cq = far_ld<L_CS + 1024 + cq_at(f), u32x2>(c);
          side(fi);
          sfor<PL.first[2 * f], PL.first[2 * f + 2]>(gop);
          both(fi);
          if constexpr (dg_at(f) >= 0) STQ(dg_at(f));
          PIN();
        });
        sfor<280, 415>([&](auto fi) {
          constexpr int f = decltype(fi)::v, st = (f - 280) / 5, jn = (f - 280) % 5;
          ad[0][jn] = mm16p(W[f], B[st][0], st ? ad[0][jn] : z4);
          ahead(fi);
          side(fi);
          sfor<PL.first[2 * f], PL.first[2 * f + 1]>(gop);
          PIN();
          ad[1][jn] = mm16p(W[f], B[st][1], st ? ad[1][jn] : z4);
          sfor<PL.first[2 * f + 1], PL.first[2 * f + 2]>(gop);
          both(fi);
          if constexpr (dg_at(f) >= 0) STQ(dg_at(f));
          PIN();
        });
        epi(ad, 1);
        STF(0, 11); STF(1, 16); STF(2, 17); STF(3, 18); STF(4, 19); STF(5, 20); STF(6, 21); STF(7, 12); STF(8, 22); STF(9, 13); STF(10, 23); STF(11, 14); ST(15);
      }
    } else {
      LANES();
      if (wq < 2) {
        STR_INIT(24);
        __builtin_amdgcn_s_setprio(HPRIO);
        const u32 wg = OFF_WG + s * 286720, wd = OFF_WD + cs * 276480;
        u32x4 R[RGU * 5 + 1];
        auto rld = [&](auto i) { constexpr int k = decltype(i)::v; R[k] = __builtin_amdgcn_raw_buffer_load_b128(rs, c.l16 + (k % 5) * 1024, wd + (27 - RGU + k / 5) * UN + t.hl, 0);
          if constexpr (k % 5 == 4) { PIN(); WAITV(NR); PIN(); } };
        stream<NC>(t, OFF_WO + cs * 102400 + 8 * UN, 1, 2, eb + 17, 0);
        stream<NC>(t, wg, 3, 3, eb + 17, 0);
        sfor<0, RG1 * 5>(rld);
        stream<NC>(t, wg + 5 * UN, 8, 10, eb + 18, 1);
        sfor<RG1 * 5, RG2 * 5>(rld);
        stream<NC>(t, wg + 3 * UN, 6, 2, eb + 29, 0);
        sfor<RG2 * 5, RGU * 5>(rld);
        STH(16);
        stream<NB>(t, wg + 15 * UN, 3, 1, eb + 30 + GPAUSE, 1);
        stream<NB>(t, wg + 16 * UN, 4, 12, eb + 31, 1);
        STH(17);
        stream<NB>(t, wd, 1, 27 - RGU, eb + 43, 1);
        if (RGU) WAITV(0);
        sfor<0, RGU>([&](auto ii) { constexpr int i = decltype(ii)::v, u = 27 - RGU + i;
          spin_ge<1>(t.fa, eb + 43 + u);
          for (int j = 0; j < 5; ++j) lds_st<u32x4>(((1 + u) % 15) * UN + t.hl + j * 1024 + c.l16, R[i * 5 + j]);
          asm volatile("" ::: "memory");
          lds_post(t.fl + lane * 4, fb + 55 + u); });
        t.seq += 4 * RGU;
        STH(18);
        if (layer == 0) {
          stream<NC>(t, OFF_WQ + s * 163840, 0, 1, QPAUSE, 1);
          stream<NC>(t, OFF_WQ + s * 163840 + UN, 1, 12, 73, 1);
          stream<NC>(t, OFF_WQ + s * 163840 + 13 * UN, 13, 2, 86, 0);
        }
        __builtin_amdgcn_s_setprio(0);
        STH(19);
      } else {
        const u32 fa = L_FREE + MBW(lane) + (lane & 3) * 4;
        const u32 pa = L_FULL + MBW(g) + (wq & 1) * 64 + l15 * 4;
        if (wq == 2) {
          constexpr unsigned long long TB = (T_EV(0) - 1) | (T_EV(1) - 1) << 8 | (T_EV(2) - 1) << 16 | (T_EV(3) - 1) << 24 | (unsigned long long)(T_EV(4) - 1) << 32 | (unsigned long long)(LS + T_EV(0) - 1) << 40;
          GAS u32* p = (GAS u32*)(ws + fl + F_H * 128 + (g & 1) * HLS);
          spin_ge<2>(fa, eb + fr_cnt(PL.sig[0] + 1 + GP) + PDLY);
          ST(22);
          int n = POLL_MAX, mo = 0; (void)mo;
#pragma unroll 1
          for (; n > 0; --n) {
            const int m = __builtin_popcount(__hip_atomic_load(p, RLX_AGENT) & 0x88888u);
            lds_post(pa, fb + (int)((TB >> (8 * m)) & 255));
            if (__builtin_amdgcn_ballot_w64(m < 5) == 0) break;
            __builtin_amdgcn_s_sleep(PSLP);
          }
          if (n == 0) lds_post(pa, fb + LS + T_EV(0) - 1);
          ST(23);
        }
        if (wq == 3) {
          GAS u32* p = (GAS u32*)(ws + gfl + F_H * 128 + (s >> 3) * HLS + l15 * 8192);
          sfor<0, 5>([&](auto ei) { constexpr int e = decltype(ei)::v;
            spin_ge<1>(fa, eb + fr_cnt(PL.sig[e] + 1 + GP));
            if (lane < 16) (void)__hip_atomic_fetch_add(p, vk<1u << (4 * e)>(), RLX_AGENT);
          });
        }
        if (wq == 3) { spin_ge<2>(fa, eb + 71); lds_post(pa, fb + LS + T_KV - 1); }
      }
    }
  }
  WAITV(0);
}

DEV void dma1(rsrc_t r, u32 vo, u32 ld) { asm volatile("s_mov_b32 m0, %2\n\ts_nop 0\n\tbuffer_load_dwordx4 %0, %1, 0 offen lds" :: "v"(vo), "s"(r), "s"(ld) : "memory"); }
constexpr int tile0(int s) { return (s >> 3) * 54 + ((s & 7) < 7 ? 7 * (s & 7) : 48); }
__global__ void __launch_bounds__(512) prep(const unsigned short* __restrict__ x, const unsigned short* __restrict__ wqkv, const unsigned short* __restrict__ wo,
                                            const unsigned short* __restrict__ wg, const unsigned short* __restrict__ wu, const unsigned short* __restrict__ wd,
                                            unsigned char* __restrict__ ws) {
  const int u = blockIdx.x, tid = threadIdx.x, lane = tid & 63, wv = __builtin_amdgcn_readfirstlane(tid >> 6);
  const int l31 = lane & 31, hh = lane >> 5, l15 = lane & 15, g = lane >> 4, q = l15 >> 2, p = l15 & 3;
  const u32 lb = wv * 8192;
  PST(0);
  if (u < 256 && tid < 256) *(u32x4*)(ws + OFF_FLAG + u * 8192 + tid * 16) = u32x4{0u, 0u, 0u, 0u};
  if (u == 257 && tid < 384) { const u32 b = tid < 320 ? 0u : (tid - 320) * 4, m = tid < 320 ? 0u : 1u; *(u32x4*)(ws + OFF_ZERO + tid * 16) = u32x4{b, b + m, b + 2 * m, b + 3 * m}; }
  if (u < 64) {
    u32x4 v[5];
    for (int k = 0; k < 5; ++k) v[k] = *(const u32x4*)(x + (size_t)(u * 32 + l31) * 640 + (wv * 5 + k) * 16 + hh * 8);
    float sm = 0, sq = 0;
    for (int k = 0; k < 5; ++k) {
      *(u32x4*)(ws + OFF_X + u * XT + (wv * 5 + k) * 1024 + lane * 16) = v[k];
      for (int j = 0; j < 4; ++j) { sm = dot2(v[k][j], 0x3f803f80u, sm); sq = dot2(v[k][j], v[k][j], sq); } }
    sm = swap32_add(sm); sq = swap32_add(sq);
    if (hh == 0) *(f32x2*)(ws + OFF_X + u * XT + 40960 + wv * 256 + l31 * 8) = f32x2{sm, sq};
  } else if (u < 138) {
    const bool isO = u < 84;
    const int v = isO ? u - 64 : u - 84, kh = isO ? v / 10 : v / 27, st = isO ? v % 10 : v % 27;
    const rsrc_t r = __builtin_amdgcn_make_buffer_rsrc((void*)(isO ? wo : wd), 0, isO ? 640 * 1280 : 1728 * 1280, 0x00020000);
    for (int i = 0; i < 5; ++i) { const int pc = i * 64 + lane, sl = pc / 10, e = pc % 10, gg = sl >> 3, j = sl & 7, d = 8 * (j >> 2) + 4 * (gg & 1) + (j & 3);
      int row;
      if (isO) row = st < 8 ? (kh * 8 + st) * 40 + (gg >> 1) * 20 + d : (kh * 8 + (st - 8) * 4 + gg) * 40 + (j >> 2) * 20 + 16 + (j & 3);
      else { const int s7 = (st & 3) * 2 + (gg >> 1); row = (kh * 54 + (s7 < 7 ? 7 * s7 : 48) + (st >> 2)) * 16 + d; }
      dma1(r, row * 1280 + wv * 160 + e * 16, lb + i * 1024); }
    WAITV(0); PST(1);
    unsigned char* dst = ws + (isO ? OFF_WO + wv * 102400 : OFF_WD + wv * 276480) + st * UN + kh * 5120 + lane * 16;
    for (int jn = 0; jn < 5; ++jn) { const u32 a = lb + (8 * g + q) * 160 + (jn < 4 ? (jn >> 1) * 32 + 8 * p + 4 * (jn & 1) : 64 + 4 * p) * 2;
      const u32x2 lo = lds_tr(a), hi = lds_tr(a + 640);
      *(u32x4*)(dst + jn * 1024) = u32x4{lo[0], lo[1], hi[0], hi[1]}; }
  } else {
    const bool isG = u < 218;
    const int v = isG ? u - 138 : u - 218, kb = v % 5, sb = v / 5, ks = kb * 8 + wv;
    float cs[8];
    if (isG) {
      const int t0 = (sb >> 3) * 54 + ((sb & 7) < 7 ? 7 * (sb & 7) : 48), nt = (sb & 7) < 6 ? 7 : 6;
      const rsrc_t rg = __builtin_amdgcn_make_buffer_rsrc((void*)wg, 0, 640 * 3456, 0x00020000), ru = __builtin_amdgcn_make_buffer_rsrc((void*)wu, 0, 640 * 3456, 0x00020000);
      for (int i = 0; i < 4; ++i) { const int e = l15 < 2 * nt ? l15 : 0; const u32 vo = (ks * 16 + i * 4 + g) * 3456 + t0 * 32 + e * 16;
        dma1(rg, vo, lb + i * 1024); dma1(ru, vo, lb + 4096 + i * 1024); }
      WAITV(0); PST(1);
      for (int t = 0; t < 7; ++t) { const u32 a = lb + (g & 1) * 4096 + (8 * hh + q) * 256 + t * 32 + p * 8;
        const u32x2 lo = lds_tr(a), hi = lds_tr(a + 1024);
        const int f = t < 3 ? (ks < K1 ? ks * 3 + t : 2 * K1 + t * (40 - K1) + ks) : t * 40 + ks;
        *(u32x4*)(ws + OFF_WG + sb * 286720 + f * 1024 + lane * 16) = u32x4{lo[0], lo[1], hi[0], hi[1]};
        cs[t] = dot2(lo[0], 0x3f803f80u, dot2(lo[1], 0x3f803f80u, dot2(hi[0], 0x3f803f80u, dot2(hi[1], 0x3f803f80u, 0.f)))); }
      cs[7] = 0.f;
    } else {
      const rsrc_t r = __builtin_amdgcn_make_buffer_rsrc((void*)wqkv, 0, 640 * 3840, 0x00020000);
      for (int i = 0; i < 8; ++i) { const int e = l31 < 30 ? l31 : 0; dma1(r, (ks * 16 + i * 2 + hh) * 3840 + (e / 10) * 1280 + sb * 160 + (e % 10) * 16, lb + i * 1024); }
      WAITV(0); PST(1);
      for (int t = 0; t < 8; ++t) { const int tt = t & 3, gam = (g & 1) * 4 + p;
        const int col = tt == 2 ? (gam < 2 ? 80 : gam < 4 ? 0 : 160) + (gam & 1) * 20 + 16 : (tt == 0 ? 80 : tt == 1 ? 160 : 0) + (g & 1) * 20 + p * 4;
        const u32 a = lb + (8 * hh + q) * 512 + ((t >> 2) * 40 + col) * 2;
        const u32x2 lo = lds_tr(a), hi = lds_tr(a + 2048);
        *(u32x4*)(ws + OFF_WQ + (sb * 2 + (t >> 2)) * 163840 + (tt < 3 ? ks * 3 + tt : 120 + ks) * 1024 + lane * 16) = u32x4{lo[0], lo[1], hi[0], hi[1]};
        cs[t] = dot2(lo[0], 0x3f803f80u, dot2(lo[1], 0x3f803f80u, dot2(hi[0], 0x3f803f80u, dot2(hi[1], 0x3f803f80u, 0.f)))); }
    }
    asm volatile("" ::: "memory");
    for (int t = 0; t < 8; ++t) lds_st<float>(lb + t * 256 + lane * 4, cs[t]);
    BAR();
    float a = 0;
    for (int i = 0; i < 8; ++i) a += lds_ld<float>(i * 8192 + wv * 256 + lane * 4);
    a = swap32_add(a);
    const int s = isG ? sb : sb * 2 + (wv >> 2), tile = isG ? 4 + wv : wv & 3;
    if (hh == 0) ((float*)(ws + OFF_CS))[(s * 5 + kb) * 384 + tile * 32 + l31] = a;
  }
  PST(2);
}

#if defined(__HIP_DEVICE_COMPILE__)
#pragma clang attribute pop
#endif
extern "C" void kernel_launch(void* const* d_in, const int* in_sizes, int n_in, void* d_out, int out_size, void* d_ws, size_t ws_size, hipStream_t stream) {
  (void)in_sizes; (void)n_in; (void)out_size; (void)ws_size;
  (void)hipFuncSetAttribute((const void*)mega, hipFuncAttributeMaxDynamicSharedMemorySize, LDS_BYTES);
  prep<<<258, 512, 65536, stream>>>((const unsigned short*)d_in[0], (const unsigned short*)d_in[1], (const unsigned short*)d_in[2], (const unsigned short*)d_in[4], (const unsigned short*)d_in[5], (const unsigned short*)d_in[6], (unsigned char*)d_ws);
  mega<<<256, 512, LDS_BYTES, stream>>>((unsigned short*)d_out, (unsigned char*)d_ws);
}
```

```cpp
#define APRIO 1
#define GSLP 4
#define DEFER 0
#define EB 0
#include <hip/hip_runtime.h>
#include <stdint.h>
#if defined(__HIP_DEVICE_COMPILE__)
#pragma clang attribute push(__attribute__((target("no-packed-fp32-ops"))), apply_to = function)
#endif

typedef unsigned u32;
typedef __attribute__((ext_vector_type(4))) unsigned u32x4;
typedef __attribute__((ext_vector_type(2))) unsigned u32x2;
typedef __attribute__((ext_vector_type(8))) __bf16 bf16x8;
typedef __attribute__((ext_vector_type(2))) __bf16 bf16x2;
typedef __attribute__((ext_vector_type(4))) short s16x4;
typedef __attribute__((ext_vector_type(2))) float f32x2;
typedef __attribute__((ext_vector_type(4))) float f32x4;
typedef __attribute__((ext_vector_type(16))) float f32x16;
#define LAS __attribute__((address_space(3)))
#define GAS __attribute__((address_space(1)))
#define RLX_AGENT __ATOMIC_RELAXED, __HIP_MEMORY_SCOPE_AGENT
#define RLX_WG __ATOMIC_RELAXED, __HIP_MEMORY_SCOPE_WORKGROUP
#define DEV __device__ __forceinline__
typedef __amdgpu_buffer_rsrc_t rsrc_t;

constexpr u32 KVT = 5120;
constexpr u32 XT = 43008;
constexpr u32 HG = 55296;
constexpr u32 OFF_SLOT = 0;
constexpr u32 OFF_EP = 262144;
constexpr u32 OFF_FLAG = 1u << 20;
#define SAUX 16
constexpr u32 OFF_WQ = 3u << 20;
constexpr u32 OFF_WO = OFF_WQ + 16 * 163840;
constexpr u32 OFF_WG = OFF_WO + 8 * 102400;
constexpr u32 OFF_WD = OFF_WG + 16 * 286720;
constexpr u32 OFF_CS = OFF_WD + 8 * 276480;
constexpr u32 OFF_X = OFF_CS + 256 * 2048;
constexpr u32 XSZ = 64 * XT;
constexpr u32 OFF_KV = OFF_X + 4 * XSZ;
constexpr u32 KVSZ = 16 * 64 * KVT;
constexpr u32 OFF_O = OFF_KV + 2 * KVSZ;
constexpr u32 OSZ = 128 * 20480;
constexpr u32 OFF_H = OFF_O + 2 * OSZ;
constexpr u32 HSZ = 128 * HG;
constexpr u32 OFF_END = OFF_H + 2 * HSZ;
constexpr u32 OFF_STAMP = 96u << 20;
enum { F_X = 0, F_KV = 4, F_O = 5, F_X2 = 9, F_H = 13, F_TMO = 63 };

constexpr u32 UN = 10240;
constexpr u32 KVL = 5248;
constexpr u32 L_KV = UN;
constexpr u32 L_QH = L_KV + 12 * KVL;
constexpr u32 L_X1 = 6 * UN, L_X2 = 13 * UN;
constexpr u32 L_FULL = 15 * UN;
constexpr u32 L_FREE = L_FULL + 1024;
constexpr u32 L_CS = L_FREE + 2048;
constexpr u32 L_ATT = L_CS + 3072;
constexpr u32 L_QF = 163584;
constexpr u32 LDS_BYTES = 163840;
constexpr int LF = 86, LS = 81;
constexpr int T_KV = 11, T_TB = 13, T_CS = 31, T_X2 = 81;

#define POLL_MAX 0x10000
#define SPIN_MAX 0x100000
#define BAR() do { __builtin_amdgcn_sched_barrier(0); asm volatile("s_waitcnt lgkmcnt(0)\n\ts_barrier" ::: "memory"); __builtin_amdgcn_sched_barrier(0); } while (0)
#define WAITV(n) asm volatile("s_waitcnt vmcnt(%0)" :: "i"(n) : "memory")
#define PIN() __builtin_amdgcn_sched_barrier(0)

#define ST(i) do {} while (0)
#define PST(i) do {} while (0)
#define STQ(k) do {} while (0)
#define STF(k, i) do {} while (0)

#define LQ(k) do {} while (0)
#define LQA(k) LQ(k)
constexpr int ld_at(int f) { return -1; }
DEV float asf(u32 x) { return __builtin_bit_cast(float, x); }
DEV u32 asu(float x) { return __builtin_bit_cast(u32, x); }
DEV u32 pk2(float a, float b) { f32x2 v = {a, b}; bf16x2 r = __builtin_convertvector(v, bf16x2); return __builtin_bit_cast(u32, r); }
DEV float bflo(u32 v) { return __builtin_bit_cast(float, v << 16); }
DEV float bfhi(u32 v) { return __builtin_bit_cast(float, v & 0xffff0000u); }
DEV u32x4 ld16(rsrc_t r, u32 vo, u32 so) { return __builtin_amdgcn_raw_buffer_load_b128(r, vo, so, 16); }
DEV u32x2 ld8(rsrc_t r, u32 vo, u32 so) { return __builtin_amdgcn_raw_buffer_load_b64(r, vo, so, 16); }
DEV void st16(u32x4 v, rsrc_t r, u32 vo, u32 so) { __builtin_amdgcn_raw_buffer_store_b128(v, r, vo, so, SAUX); asm volatile("s_nop 1" :: "v"(v)); }
template <typename T> DEV T lds_ld(u32 a) { return *(LAS T*)a; }
template <typename T> DEV void lds_st(u32 a, T v) { *(LAS T*)a = v; }
DEV u32x2 lds_tr(u32 a) { return __builtin_bit_cast(u32x2, __builtin_amdgcn_ds_read_tr16_b64_v4i16((LAS s16x4*)a)); }
DEV int lds_word(u32 a) { return __hip_atomic_load((LAS int*)a, RLX_WG); }
DEV void lds_post(u32 a, int v) { __hip_atomic_store((LAS int*)a, v, RLX_WG); }
DEV f32x16 mm32(u32x4 a, u32x4 b, f32x16 c) { return __builtin_amdgcn_mfma_f32_32x32x16_bf16(__builtin_bit_cast(bf16x8, a), __builtin_bit_cast(bf16x8, b), c, 0, 0, 0); }
DEV f32x16 mm32p(u32x4 a, u32x4 b, f32x16 c) { f32x16 r = mm32(a, b, c); asm volatile("" : "+v"(r)); return r; }
DEV f32x16 mm32k8(u32x2 a, u32x2 b, f32x16 c) { return __builtin_amdgcn_mfma_f32_32x32x8bf16_1k(__builtin_bit_cast(s16x4, a), __builtin_bit_cast(s16x4, b), c, 0, 0, 0); }
DEV f32x16 mm32k8p(u32x2 a, u32x2 b, f32x16 c) { f32x16 r = mm32k8(a, b, c); asm volatile("" : "+v"(r)); return r; }
DEV f32x4 mm16(u32x4 a, u32x4 b, f32x4 c) { return __builtin_amdgcn_mfma_f32_16x16x32_bf16(__builtin_bit_cast(bf16x8, a), __builtin_bit_cast(bf16x8, b), c, 0, 0, 0); }
DEV f32x4 mm16p(u32x4 a, u32x4 b, f32x4 c) { f32x4 r = mm16(a, b, c); asm volatile("" : "+v"(r)); return r; }
DEV void mm16t(u32x4 a, u32x4 b, f32x4& c) { asm volatile("v_mfma_f32_16x16x32_bf16 %0, %1, %2, %0" : "+v"(c) : "v"(a), "v"(b)); }
DEV float swap32_max(float v) { u32 x = asu(v); auto r = __builtin_amdgcn_permlane32_swap(x, x, false, false); return fmaxf(asf(r[0]), asf(r[1])); }
DEV float swap32_add(float v) { u32 x = asu(v); auto r = __builtin_amdgcn_permlane32_swap(x, x, false, false); return asf(r[0]) + asf(r[1]); }
DEV float swap16_add(float v) { u32 x = asu(v); auto r = __builtin_amdgcn_permlane16_swap(x, x, false, false); return asf(r[0]) + asf(r[1]); }
DEV float dot2(u32 a, u32 b, float c) { return __builtin_amdgcn_fdot2_f32_bf16(__builtin_bit_cast(bf16x2, a), __builtin_bit_cast(bf16x2, b), c, false); }
DEV u32 split2(float m) { const u32 ph = pk2(m, 0.f) & 0xffffu; return ph | (pk2(m - bflo(ph), 0.f) << 16); }
DEV u32 mulpk(float a, float b, float r) { u32 p, t; asm volatile("v_mul_f32 %0, %2, %4\n\tv_mul_f32 %1, %3, %4\n\tv_cvt_pk_bf16_f32 %0, %0, %1" : "=&v"(p), "=&v"(t) : "v"(a), "v"(b), "v"(r)); return p; }
DEV float xmul(float a, float b) { float r; asm volatile("v_mul_f32 %0, %1, %2" : "=v"(r) : "v"(a), "v"(b)); return r; }
DEV float xfma(float a, float b, float c) { float r; asm volatile("v_fma_f32 %0, %1, %2, %3" : "=v"(r) : "v"(a), "v"(b), "v"(c)); return r; }
DEV float xexp(float a) { float r; asm volatile("v_exp_f32 %0, %1" : "=v"(r) : "v"(a)); return r; }
DEV float xrcp(float a) { float r; asm volatile("v_rcp_f32 %0, %1" : "=v"(r) : "v"(a)); return r; }
DEV u32 xpk(float a, float b) { u32 r; asm volatile("v_cvt_pk_bf16_f32 %0, %1, %2" : "=v"(r) : "v"(a), "v"(b)); return r; }
DEV float xlo(u32 a) { float r; asm volatile("v_lshlrev_b32 %0, 16, %1" : "=v"(r) : "v"(a)); return r; }
DEV float xhi(u32 a) { float r; asm volatile("v_and_b32 %0, 0xffff0000, %1" : "=v"(r) : "v"(a)); return r; }

DEV void kvtile(GAS unsigned char* ws, u32 fo, u32& v, u32 pre, u32 bit, rsrc_t r, u32 vo, u32 so, u32 ld, u32 one) {
  u32 t;
  asm volatile("s_bitcmp1_b32 %[v], %[b]\n\ts_mov_b32 %[t], %[m]\n\ts_cbranch_scc1 1f\n0:\n\ts_load_dword %[v], %[ws], %[fo] glc\n\ts_sub_u32 %[t], %[t], 1\n\ts_waitcnt lgkmcnt(0)\n\ts_cbranch_scc1 1f\n\t"
               "s_or_b32 %[v], %[v], %[pre]\n\ts_bitcmp1_b32 %[v], %[b]\n\ts_cbranch_scc0 0b\n1:\n\t"
               "s_add_u32 m0, %[ld], 0x1080\n\ts_add_u32 %[t], %[so], 0x1000\n\t"
               "buffer_load_dwordx4 %[vo], %[r], %[t] offen sc1 lds\n\t"
               "s_mov_b32 m0, %[ld]\n\ts_nop 0\n\t"
               "buffer_load_dwordx4 %[vo], %[r], %[so] offen sc1 lds\n\t"
               "buffer_load_dwordx4 %[vo], %[r], %[so] offen offset:1024 sc1 lds\n\t"
               "buffer_load_dwordx4 %[vo], %[r], %[so] offen offset:2048 sc1 lds\n\t"
               "buffer_load_dwordx4 %[vo], %[r], %[so] offen offset:3072 sc1 lds\n\t"
               "s_add_u32 m0, %[ld], 0x1004\n\ts_mov_b64 exec, 1\n\tbuffer_load_dword off, %[r], %[one] lds\n\ts_mov_b64 exec, -1"
               : [t] "=&s"(t), [v] "+s"(v) : [ws] "s"(ws), [fo] "s"(fo), [pre] "s"(pre), [b] "s"(bit), [vo] "v"(vo), [r] "s"(r), [so] "s"(so), [ld] "s"(ld), [one] "s"(one), [m] "i"(POLL_MAX) : "memory", "scc");
}
template <typename T> DEV T anyv() { T r; asm volatile("" : "=v"(r)); return r; }
DEV void usev(u32x4 x) { asm volatile("" :: "v"(x)); }
DEV void usev2(u32x4 x, u32x4 y) { asm volatile("" :: "v"(x), "v"(y)); }
template <u32 K> DEV u32 vk() { u32 r; asm volatile("v_mov_b32 %0, %1" : "=v"(r) : "i"(K)); return r; }
#define OPQV(x) asm volatile("" : "+v"(x))
#define OPQS(x) asm volatile("" : "+s"(x))

#define SLPX 1
template <int SLP> DEV void spin_ge(u32 a, int thr) {
  u32 t, n;
  asm volatile("s_mov_b32 %1, %5\n0:\n\tds_read_b32 %0, %2\n\ts_sub_u32 %1, %1, 1\n\ts_waitcnt lgkmcnt(0)\n\ts_cbranch_scc1 1f\n\tv_cmp_gt_i32 vcc, %3, %0\n\ts_cbranch_vccz 1f\n\ts_sleep %4\n\ts_branch 0b\n1:"
               : "=&v"(t), "=&s"(n) : "v"(a), "s"(thr), "i"(SLP * SLPX), "i"(SPIN_MAX) : "memory", "scc", "vcc");
}
typedef unsigned long long u64;
DEV u64 pre(int v, int thr) { u64 m; asm volatile("v_cmp_gt_i32 %0, %1, %2" : "=s"(m) : "s"(thr), "v"(v)); return m; }
DEV void chk(u64 m, u32 a, int thr) { if (__builtin_expect(m != 0, 0)) spin_ge<0>(a, thr); }
DEV void chk(int v, u32 a, int thr) { if (__builtin_expect(__builtin_amdgcn_ballot_w64(v < thr) != 0, 0)) spin_ge<0>(a, thr); }

DEV void sig(GAS unsigned char* ws, u32 so, u32 vo) { (void)__hip_atomic_fetch_add((GAS u32*)(ws + so + vo), vk<1>(), RLX_AGENT); }
template <u32 MASK> DEV void sigx(GAS unsigned char* p, u32 vo, u32 one) {
  asm volatile("s_mov_b64 exec, %3\n\ts_waitcnt vmcnt(0)\n\tglobal_atomic_add %0, %1, %2\n\ts_mov_b64 exec, -1" :: "v"(vo), "v"(one), "s"(p), "i"(MASK) : "memory"); }
#define SPOLL 15
template <int SLP> DEV void poll_ge(GAS unsigned char* ws, u32 so, u32 tgt) {
  if (SPOLL & 1) { u32 v, n;
    asm volatile("s_mov_b32 %1, %5\n0:\n\ts_load_dword %0, %2, %3 glc\n\ts_sub_u32 %1, %1, 1\n\ts_waitcnt lgkmcnt(0)\n\ts_cbranch_scc1 1f\n\ts_cmp_lt_u32 %0, %4\n\ts_cbranch_scc1 0b\n1:"
                 : "=&s"(v), "=&s"(n) : "s"(ws), "s"(so), "s"(tgt), "i"(POLL_MAX) : "memory", "scc");
    return; }
  GAS u32* p = (GAS u32*)(ws + so + vk<0>());
#pragma unroll 1
  for (int n = POLL_MAX; n > 0; --n) {
    if (__builtin_amdgcn_readfirstlane(__hip_atomic_load(p, RLX_AGENT)) >= tgt) break;
    if (SLP) __builtin_amdgcn_s_sleep(SLP);
  }
  __builtin_amdgcn_fence(__ATOMIC_ACQUIRE, "wavefront");
  asm volatile("" ::: "memory");
}

#define PDV 4
constexpr int PD = PDV;
constexpr int ALA = 12;

template <int F> struct IC { static constexpr int v = F; };
template <typename T, T... I> struct Seq {};
template <int B, typename Fn, int... I> DEV void sfor_(Fn&& fn, Seq<int, I...>) { const int d[] = {0, (fn(IC<B + I>{}), 0)...}; (void)d; }
template <int B, int E, typename Fn> DEV void sfor(Fn&& fn) { sfor_<B>(fn, __make_integer_seq<Seq, int, E - B>{}); }

struct Ctx { u32 l16, l16h, l16t, mb; };
template <u32 OFF, typename T> DEV T far_ld(const Ctx& c) { return OFF < 65536 ? lds_ld<T>(c.l16 + OFF) : OFF < 131072 ? lds_ld<T>(c.l16h + (OFF - 65536)) : lds_ld<T>(c.l16t + (OFF - 131072)); }
DEV void freed(const Ctx& c) { (void)__builtin_amdgcn_atomic_inc32((LAS u32*)(c.mb + 1024), c.l16h, __ATOMIC_RELAXED, "workgroup"); }

constexpr int K1 = 26;
constexpr int gu_tile(int f) { return f < 3 * K1 ? f % 3 : f < 120 ? (f - 3 * K1) / (40 - K1) : f / 40; }
constexpr int gu_ks(int f) { return f < 3 * K1 ? f / 3 : f < 120 ? K1 + (f - 3 * K1) % (40 - K1) : f % 40; }
constexpr int gu_end(int t) { return t < 3 ? 3 * K1 + (t + 1) * (40 - K1) : 40 * t + 40; }
constexpr int a_at(int f) { for (int k = ALA; k < 40; ++k) if ((k - ALA) * 5 / 2 == f) return k; return -1; }
constexpr int ag_at(int f) { for (int k = ALA; k < 40; ++k) if ((k - ALA) * 9 / 4 == f) return k; return -1; }
#define GP 5
#define WEV 2
#define DRAIN 20
#define CAPD 4
#define CAPG 5
#define GPAUSE 6
#define QPAUSE 79
#define WN 5
#define RP 29
constexpr int NCK = 15;
constexpr int CK_N[NCK + 1] = {30, 50 + 10 * WN, 120, 150, 170, 200, 230, 250, 280, 295, 315, 335, 355, 375, 395, 415};
constexpr int ck_thr(int i) { return CK_N[i + 1] <= 280 ? 26 + CK_N[i + 1] / 10 : 54 + (CK_N[i + 1] - 280) / 5; }
constexpr int ck_at(int n) { for (int i = 0; i < NCK; ++i) if (CK_N[i] == n) return i; return -1; }
constexpr int EV_CK[5] = {7, 9, 11, 12, 13};
constexpr int EV_TILE[5] = {1, 3, 4, 5, 6};
constexpr int T_EV(int e) { return ck_thr(EV_CK[e]); }
constexpr int hb_time(int b) { const int e = b < 16 ? 0 : b < 32 ? 1 : b / 8 - 2, t0 = CK_N[EV_CK[e]] - GP, i = b < 32 ? b % 16 : b % 8; return t0 + (e == 1 || e == 4 ? i * 3 / 2 : i * 2); }
constexpr int hb_at(int tau) { for (int b = 0; b < 54; ++b) if (hb_time(b) == tau) return b; return -1; }
constexpr int T_RES = 322, T_ADD = 362, T_EP = 378;
constexpr int cq_at(int f) { return f == 55 ? 0 : f == 64 || f == 112 ? 8 : f == 153 || f == 193 ? 1024 : f == 233 ? 1032 : -1; }
#define NFL 4
constexpr bool fr_at(int n) { return n < 415 && (n % 10 == 9 || (n >= 280 && n % 5 == 4)); }
constexpr int fr_cnt(int n) { return n < 280 ? 29 + (n + 1) / 10 : 57 + (n - 279) / 5; }
struct Plan { int op[7 * 69], first[833], st[7], sig[5], last[7]; };
constexpr int fixed_ops(int tau) { const int n = tau + GP; int c = 1;
  if (tau % WEV == WEV - 1) ++c;
  if (ck_at(n) >= 0) c += 2;
  if (ck_at(n + 1) >= 0) c += 2;
  if (ck_at(n + 6) >= 0) ++c;
  if (n % 10 == 9) ++c;
  if (hb_at(tau) >= 0) c += hb_at(tau) % 8 < 2 ? 2 : 1;
  if (cq_at(tau) >= 0) ++c;
  return c; }
constexpr Plan make_plan() { Plan p{}; int o = 0;
  int stage[56] = {}, prod[56] = {}, pk[28] = {}, sto[7] = {};
  int done[56] = {};
  constexpr int CH[7] = {0, 1, 2, 3, 5, 6, 7};
  for (int e = 0; e < 5; ++e) p.sig[e] = -1;
  for (int q = 0; q < 832; ++q) { p.first[q] = o;
    const int tau = q / 2;
    if (tau < 280 && (q & 1)) continue;
    int cap = tau < 280 ? CAPG - fixed_ops(tau) : CAPD;
    for (int e = 0; e < 5; ++e) if (p.sig[e] == tau && !(q & 1)) cap -= 1;
    int stile = -1;
    for (int t = 0; t < 7; ++t) if (!sto[t] && pk[4 * t] && pk[4 * t + 1] && pk[4 * t + 2] && pk[4 * t + 3] && cap >= 2 && (tau < 280 || (q & 1))) { stile = t; cap -= 2; break; }
    for (int i = 0; i < 28; ++i) if (!pk[i] && done[2 * i] && done[2 * i + 1] && cap >= 1 && (i < 4 || sto[i / 4 - 1])) { pk[i] = 1; --cap; p.op[o++] = i / 4 * 128 + 64 + i % 4; }
    int fl = 0;
    for (int i = 0; i < 56; ++i) if (stage[i] > 0 && !pk[i / 2]) ++fl;
    for (int i = 0; i < 56; ++i) { const int t = i / 8;
      if (stage[i] == 7 || tau < gu_end(t) + 1) continue;
      if (stage[i] == 0 && fl >= (tau < 270 ? NFL : 8)) break;
      const int k = CH[stage[i]], c = k == 3 || k == 6 ? 2 : 1;
      const bool hadp = prod[i];
      if (stage[i] >= 3 && !prod[i] && cap >= 1 && (k == 7 || cap != c)) { prod[i] = 1; --cap; p.op[o++] = t * 128 + 32 + i % 8; p.last[t] = tau; }
      if (cap >= c && (k != 7 || hadp)) { if (!stage[i]) ++fl; ++stage[i]; cap -= c; p.op[o++] = t * 128 + k * 8 + i % 8; } }
    for (int i = 0; i < 56; ++i) done[i] = stage[i] == 7;
    if (stile >= 0) { sto[stile] = 1; p.op[o++] = stile * 128 + 72; p.st[stile] = tau; for (int e = 0; e < 5; ++e) if (EV_TILE[e] == stile) { int x = tau + DRAIN; while (!fr_at(x + 1 + GP)) ++x; p.sig[e] = x; } } }
  p.first[832] = o; return p; }
constexpr Plan PL = make_plan();
static_assert(PL.first[820] == 7 * 69 && PL.sig[4] < 410 && GP == 5 && PL.last[0] < 120 && PL.last[1] < 160 && PL.last[2] < 200 && PL.last[3] < 240 && PL.first[832] == 7 * 69, "accumulator reuse");
constexpr int sig_at(int tau) { for (int e = 0; e < 5; ++e) if (PL.sig[e] == tau) return e; return -1; }
constexpr int vm_after(int t0, int t1) {
  int n = 0;
  for (int x = t0 + 1; x < t1; ++x) { for (int t = 0; t < 7; ++t) if (PL.st[t] == x) ++n;
    if (hb_at(x) >= 0) ++n;
    if (x == T_RES) n += 3; }
  return n;
}

constexpr int DG[11] = {0, 45, 90, 140, 210, 250, 279, 299, 349, 379, 414};
constexpr int dg_at(int f) { for (int i = 0; i < 11; ++i) if (DG[i] == f) return i + 1; return -1; }
constexpr int qd_at(int f) { return -1; }
#define MBW(w) (512 + ((w) & 1) * 256 + ((w) >> 1 & 1) * 128)
#define SCAL() u32 pk_ = pk; OPQS(pk_); const u32 rb = pk_ & 15, s = (pk_ >> 4) & 15, wq = (pk_ >> 8) & 3, w = (pk_ >> 8) & 7, hd = w >> 2, layer = pk_ >> 12, cs = s >> 1, half = s & 1; \
  const u32 gfl = OFF_FLAG + rb * 131072 + layer * 2048, fl = gfl + s * 8192; \
  const int fb = layer * LS, eb = layer * LF; \
  const u32 G = rb * 8 + half * 4 + wq, xe_s = OFF_X + (G >> 1) * XT + 5 * cs * 1024 + (G & 1) * 256, kvb = OFF_KV + layer * KVSZ + s * 64 * KVT; \
  (void)fl; (void)hd; (void)xe_s; (void)kvb; (void)fb; (void)eb;
#define LANES() int lane; asm volatile("s_nop 2\n\tv_mbcnt_lo_u32_b32 %0, -1, 0\n\tv_mbcnt_hi_u32_b32 %0, -1, %0" : "=v"(lane)); const int l31 = lane & 31, hh = lane >> 5, l15 = lane & 15, g = lane >> 4; (void)l31; (void)hh; (void)l15; (void)g; \
  SCAL(); \
  Ctx c; c.l16 = lane * 16; c.l16h = lane * 16 + 65536; c.l16t = lane * 16 + 131072; c.mb = L_FULL + (-hh & (MBW(wq) - 128)) + (lane & 16 & (hh - 1)) * 16 + lane * 4; OPQV(c.l16h); OPQV(c.l16t); OPQV(c.mb);

struct Stat { u32x2 sp[4]; float a, b, vv, rstd; u32 mo; };
constexpr int st_at(int f) { constexpr int T[8] = {41, 43, 44, 46, 48, 50, 52, 53}; for (int i = 0; i < 8; ++i) if (T[i] == f) return i; return -1; }
template <int K> DEV void stat(Stat& t) {
  if constexpr (K == 0) { t.a = asf(t.sp[0][0]) + asf(t.sp[1][0]) + asf(t.sp[2][0]) + asf(t.sp[3][0]); OPQV(t.a); }
  if constexpr (K == 1) { t.b = asf(t.sp[0][1]) + asf(t.sp[1][1]) + asf(t.sp[2][1]) + asf(t.sp[3][1]); OPQV(t.b); }
  if constexpr (K == 2) { auto r = __builtin_amdgcn_permlane32_swap(asu(t.a), asu(t.b), false, false); t.a = (asf(r[0]) + asf(r[1])) * (1.f / 640.f); t.b = t.a; OPQV(t.a); OPQV(t.b); }
  if constexpr (K == 3) { auto r = __builtin_amdgcn_permlane32_swap(asu(t.a), asu(t.b), false, false); t.a = -asf(r[0]); t.b = asf(r[1]) - t.a * t.a; OPQV(t.a); OPQV(t.b); }
  if constexpr (K == 4) { t.vv = fmaxf(t.b, 0.f) + 1e-5f; t.mo = pk2(t.a, 0.f); OPQV(t.vv); OPQV(t.mo); }
  if constexpr (K == 5) { t.rstd = __builtin_amdgcn_rsqf(t.vv); t.a -= bflo(t.mo); OPQV(t.rstd); OPQV(t.a); }
  if constexpr (K == 6) { t.mo |= pk2(t.a, 0.f) << 16; OPQV(t.mo); }
}
struct At { float cm[16]; u32 b, t, u, aK, aR, aV[4], mb, l16, o0, o1; };
template <int K> DEV void aset(At& a, const Ctx& c, u32 qt, u32 hd, u32 s7) {
  const u32 tb = L_KV + qt * KVL, l16 = c.l16;
  if constexpr (K == 0) { a.l16 = l16; a.mb = c.mb + 1024 + hd * 1024; a.b = ((l16 & 496) - (l16 >> 9) * 64) << 14; OPQV(a.b); OPQV(a.mb); }
  if constexpr (K >= 1 && K <= 8) for (int r = 2 * K - 2; r < 2 * K; ++r) asm volatile("v_add_u32 %0, %2, %1\n\tv_and_b32 %0, 0xff800000, %0" : "=&v"(a.cm[r]) : "v"(a.b), "i"(-((8 * (r >> 2) + (r & 3)) << 18)));
  if constexpr (K == 9) { a.aK = tb + hd * 1024 + l16; OPQV(a.aK); }
  if constexpr (K == 10) { u32 x = tb + 4224 + hd * 16 + l16 * 2; OPQV(x); a.aR = l16 >= 512 ? tb + 4096 : x; OPQV(a.aR); }
  if constexpr (K == 11) { a.t = (l16 >> 2 & 128) + (l16 >> 1 & 96); a.u = (l16 & 16) + (l16 >> 2 & 8); OPQV(a.t); OPQV(a.u); }
  if constexpr (K == 12) { u32 x = tb + 2048 + hd * 1024 + a.u; OPQV(x); x = (l16 & 256) ? tb + 4232 + hd * 16 : x; a.t += x; OPQV(a.t); }
  if constexpr (K == 13) { const bool k = (l16 & 304) > 256; a.aV[0] = k ? tb + 4096 : a.t; a.u = k ? 0 : vk<256>(); OPQV(a.aV[0]); OPQV(a.u); }
  if constexpr (K == 14) for (int r = 1; r < 4; ++r) { a.aV[r] = a.aV[r - 1] + a.u; OPQV(a.aV[r]); }
  if constexpr (K == 15) { a.t = l16 & 256; OPQV(a.t); a.t *= 80; a.o0 = (l16 & 240) + (l16 >> 1 & 256); OPQV(a.t); OPQV(a.o0); }
  if constexpr (K == 16) { a.o0 += a.t + s7 * 1024 + hd * 512; a.t += (l16 >> 1 & 120) + 8192 + s7 * 256 + hd * 128; OPQV(a.o0); OPQV(a.t); }
  if constexpr (K == 17) { a.o1 = (l16 & 528) ? vk<OFF_END>() : a.t; OPQV(a.o1); }
}
constexpr int NSET = 18;
template <int I> DEV void redo(f32x16& sc, u32x2 (&V)[4], u32x4& K, u32x2& R, const At& a, u32x4 qm, u32x2 qr, const f32x16& cm) {
  u32 n; u32x4 k; u32x2 r;
#define REDO(C) "s_mov_b32 %[n], %[m]\n0:\n\tds_read_b64 %[r], %[ar] offset:%[o]\n\tds_read_b128 %[k], %[ak] offset:%[o]\n\ts_sub_u32 %[n], %[n], 1\n\ts_waitcnt lgkmcnt(0)\n\ts_cbranch_scc1 1f\n\t" \
    "v_cmp_gt_u64 vcc, %[hi], %[r]\n\ts_nop 0\n\ts_cmp_lg_u32 vcc_hi, 0\n\ts_cbranch_scc1 0b\n1:\n\t" \
    "v_mfma_f32_32x32x16_bf16 %[s], %[k], %[qm], " C "\n\tds_read_b64 %[rn], %[ar] offset:%[p]\n\tds_read_b128 %[kn], %[ak] offset:%[p]\n\t" \
    "ds_read_b64_tr_b16 %[v0], %[a0] offset:%[o]\n\tds_read_b64_tr_b16 %[v1], %[a1] offset:%[o]\n\tds_read_b64_tr_b16 %[v2], %[a2] offset:%[o]\n\tds_read_b64_tr_b16 %[v3], %[a3] offset:%[o]\n\t" \
    "s_nop 7\n\tv_mfma_f32_32x32x8_bf16 %[s], %[r], %[qr], %[s]\n\ts_nop 7\n\ts_waitcnt lgkmcnt(0)"
#define REDO_O [s] "+v"(sc), [kn] "+v"(K), [rn] "+v"(R), [v0] "+v"(V[0]), [v1] "+v"(V[1]), [v2] "+v"(V[2]), [v3] "+v"(V[3]), [n] "=&s"(n), [k] "=&v"(k), [r] "=&v"(r)
#define REDO_I [ak] "v"(a.aK), [ar] "v"(a.aR), [a0] "v"(a.aV[0]), [a1] "v"(a.aV[1]), [a2] "v"(a.aV[2]), [a3] "v"(a.aV[3]), [qm] "v"(qm), [qr] "v"(qr), [hi] "s"(1ull << 32), [m] "i"(SPIN_MAX), [o] "i"((8 - I) * KVL), [p] "i"((I < 8 ? 7 - I : 0) * KVL)
  if constexpr (I < 8) asm volatile(REDO("0") : REDO_O : REDO_I : "memory", "scc", "vcc");
  else asm volatile(REDO("%[cm]") : REDO_O : REDO_I, [cm] "v"(cm) : "memory", "scc", "vcc");
}

#define STH(i) ST(i)
#define RFL(x) __builtin_amdgcn_readfirstlane(x)
struct Str { rsrc_t rs; u32 l16, fa, fl, seq, hl; };
#define OAUX 0
#define XF 1
#define XF0 "s_mov_b64 exec, 0xffff\n\t"
#define XF1 "s_mov_b64 exec, -1\n\t"
#define SC "sc1 "
template <int K> DEV void stream(Str& t, u32 so, u32 lu, int n, int thr, int dthr) {
  u32 ld = lu * UN + t.hl, fr, x, tries, seq = t.seq, m;
  so += t.hl; t.seq += 4 * n;
  so = RFL(so); ld = RFL(ld); thr = RFL(thr); seq = RFL(seq);
  asm volatile("s_mov_b32 %[n], %[n0]\n\ts_mov_b32 %[tr], %[t0]\n\ts_branch 2f\n1:\n\ts_sleep 1\n\ts_sub_u32 %[tr], %[tr], 1\n\ts_cmp_lt_i32 %[tr], 0\n\ts_cbranch_scc1 3f\n2:\n\tds_read_b32 %[fr], %[fa]\n\ts_waitcnt lgkmcnt(0)\n"
               "0:\n\tv_cmp_gt_i32 vcc, %[thr], %[fr]\n\ts_mov_b32 m0, %[ld]\n\ts_add_u32 %[x], %[so], 0x1000\n\ts_cbranch_vccnz 1b\n"
               "3:\n\tbuffer_load_dwordx4 %[la], %[r], %[so] offen " SC "lds\n\t"
               "buffer_load_dwordx4 %[la], %[r], %[so] offen offset:1024 " SC "lds\n\t"
               "buffer_load_dwordx4 %[la], %[r], %[so] offen offset:2048 " SC "lds\n\t"
               "buffer_load_dwordx4 %[la], %[r], %[so] offen offset:3072 " SC "lds\n\t"
               "s_add_u32 m0, m0, 0x1000\n\ts_add_u32 %[so], %[so], %[un]\n\t"
               "buffer_load_dwordx4 %[la], %[r], %[x] offen " SC "lds\n\t"
               "s_mov_b32 m0, %[fl]\n\ts_add_u32 %[seq], %[seq], 4\n\t" XF0
               "buffer_load_dword off, %[r], %[seq] lds\n\t" XF1
               "s_add_u32 %[ld], %[ld], %[un]\n\ts_add_u32 %[thr], %[thr], %[dt]\n\t"
               "s_cmp_ge_u32 %[ld], %[lim]\n\ts_cselect_b32 %[x], %[lim], 0\n\ts_sub_u32 %[ld], %[ld], %[x]\n\t"
               "s_sub_u32 %[n], %[n], 1\n\ts_waitcnt vmcnt(%[k])\n\ts_cbranch_scc0 0b"
               : [fr] "=&v"(fr), [x] "=&s"(x), [so] "+s"(so), [ld] "+s"(ld), [thr] "+s"(thr), [seq] "+s"(seq), [n] "=&s"(m), [tr] "=&s"(tries)
               : [fa] "v"(t.fa), [fl] "s"(RFL(t.fl)), [la] "v"(t.l16), [r] "s"(t.rs), [dt] "i"(dthr), [lim] "i"(15 * UN), [un] "i"(UN), [k] "i"(K), [n0] "i"(n - 1), [t0] "i"(SPIN_MAX) : "memory", "scc", "vcc");
}
#define WP 6
#define BLA 12
#define ROT 0
#define WPAUSE 23
#define NSTG 0
#define SG0 6
#define SG1 23
#define PFM 0
#define PF1 24
#define PF2 36
#define HLS 128
#define PDLY 2
#define PSLP 2
#define CPRIO 2
#define BPRIO 0
#define KSIG 142
#define HPRIO 0
#define NA 30
#define NB 24
#define NC 12

#define WARM 65536
#define EPOCH 1
DEV u32 call_tag(const unsigned char* wsp) { u32 e = 0;
  if (EPOCH) asm volatile("s_load_dword %0, %1, %2\n\ts_waitcnt lgkmcnt(0)" : "=s"(e) : "s"(wsp), "i"(OFF_EP) : "memory");
  return e * 0x9E3779B5u + 0x7F4A7C15u; }
DEV void put_tag(GAS unsigned char* ws, u32 off, u32 tag) { __hip_atomic_store((GAS u32*)(ws + off), tag, RLX_AGENT); }
DEV void poll_tag(GAS unsigned char* ws, u32 off, u32 tag) {
  GAS unsigned long long* p = (GAS unsigned long long*)(ws + off);
  const unsigned long long t2 = tag | (unsigned long long)tag << 32;
#pragma unroll 1
  for (int n = POLL_MAX * 16; n > 0; --n) {
    if (__builtin_amdgcn_ballot_w64(__hip_atomic_load(p, RLX_AGENT) != t2) == 0) break;
    __builtin_amdgcn_s_sleep(1);
  }
  __builtin_amdgcn_fence(__ATOMIC_ACQUIRE, "wavefront");
  asm volatile("" ::: "memory");
}
DEV void tag8(GAS unsigned char* ws, u32 so, u32 tag) { u32 n; ws += so;
  asm volatile("s_mov_b32 %[n], %[m]\n0:\n\ts_load_dwordx8 s[68:75], %[p], 0 glc\n\ts_sub_u32 %[n], %[n], 1\n\ts_waitcnt lgkmcnt(0)\n\ts_cbranch_scc1 1f\n\t"
               "s_cmp_eq_u64 s[68:69], %[t]\n\ts_cbranch_scc0 0b\n\t"
               "s_cmp_eq_u64 s[70:71], %[t]\n\ts_cbranch_scc0 0b\n\t"
               "s_cmp_eq_u64 s[72:73], %[t]\n\ts_cbranch_scc0 0b\n\t"
               "s_cmp_eq_u64 s[74:75], %[t]\n\ts_cbranch_scc0 0b\n\t"
               "\n1:" : [n] "=&s"(n) : [p] "s"(ws), [t] "s"(tag | (unsigned long long)tag << 32), [m] "i"(POLL_MAX * 16) : "memory", "scc", "s68", "s69", "s70", "s71", "s72", "s73", "s74", "s75"); }
DEV void tag32(GAS unsigned char* ws, u32 so, u32 tag) { u32 n; ws += so;
  asm volatile("s_mov_b32 %[n], %[m]\n0:\n\ts_load_dwordx16 s[68:83], %[p], 0 glc\n\ts_load_dwordx16 s[84:99], %[p], 64 glc\n\ts_sub_u32 %[n], %[n], 1\n\ts_waitcnt lgkmcnt(0)\n\ts_cbranch_scc1 1f\n\t"
               "s_cmp_eq_u64 s[68:69], %[t]\n\ts_cbranch_scc0 0b\n\t"
               "s_cmp_eq_u64 s[70:71], %[t]\n\ts_cbranch_scc0 0b\n\t"
               "s_cmp_eq_u64 s[72:73], %[t]\n\ts_cbranch_scc0 0b\n\t"
               "s_cmp_eq_u64 s[74:75], %[t]\n\ts_cbranch_scc0 0b\n\t"
               "s_cmp_eq_u64 s[76:77], %[t]\n\ts_cbranch_scc0 0b\n\t"
               "s_cmp_eq_u64 s[78:79], %[t]\n\ts_cbranch_scc0 0b\n\t"
               "s_cmp_eq_u64 s[80:81], %[t]\n\ts_cbranch_scc0 0b\n\t"
               "s_cmp_eq_u64 s[82:83], %[t]\n\ts_cbranch_scc0 0b\n\t"
               "s_cmp_eq_u64 s[84:85], %[t]\n\ts_cbranch_scc0 0b\n\t"
               "s_cmp_eq_u64 s[86:87], %[t]\n\ts_cbranch_scc0 0b\n\t"
               "s_cmp_eq_u64 s[88:89], %[t]\n\ts_cbranch_scc0 0b\n\t"
               "s_cmp_eq_u64 s[90:91], %[t]\n\ts_cbranch_scc0 0b\n\t"
               "s_cmp_eq_u64 s[92:93], %[t]\n\ts_cbranch_scc0 0b\n\t"
               "s_cmp_eq_u64 s[94:95], %[t]\n\ts_cbranch_scc0 0b\n\t"
               "s_cmp_eq_u64 s[96:97], %[t]\n\ts_cbranch_scc0 0b\n\t"
               "s_cmp_eq_u64 s[98:99], %[t]\n\ts_cbranch_scc0 0b\n\t"
               "\n1:" : [n] "=&s"(n) : [p] "s"(ws), [t] "s"(tag | (unsigned long long)tag << 32), [m] "i"(POLL_MAX * 16) : "memory", "scc", "s68", "s69", "s70", "s71", "s72", "s73", "s74", "s75", "s76", "s77", "s78", "s79", "s80", "s81", "s82", "s83", "s84", "s85", "s86", "s87", "s88", "s89", "s90", "s91", "s92", "s93", "s94", "s95", "s96", "s97", "s98", "s99"); }
DEV void tag48(GAS unsigned char* ws, u32 so, u32 tag) { u32 n; ws += so;
  asm volatile("s_mov_b32 %[n], %[m]\n0:\n\ts_load_dwordx16 s[52:67], %[p], 0 glc\n\ts_load_dwordx16 s[68:83], %[p], 64 glc\n\ts_load_dwordx16 s[84:99], %[p], 128 glc\n\ts_sub_u32 %[n], %[n], 1\n\ts_waitcnt lgkmcnt(0)\n\ts_cbranch_scc1 1f\n\t"
               "s_cmp_eq_u64 s[52:53], %[t]\n\ts_cbranch_scc0 0b\n\t"
               "s_cmp_eq_u64 s[54:55], %[t]\n\ts_cbranch_scc0 0b\n\t"
               "s_cmp_eq_u64 s[56:57], %[t]\n\ts_cbranch_scc0 0b\n\t"
               "s_cmp_eq_u64 s[58:59], %[t]\n\ts_cbranch_scc0 0b\n\t"
               "s_cmp_eq_u64 s[60:61], %[t]\n\ts_cbranch_scc0 0b\n\t"
               "s_cmp_eq_u64 s[62:63], %[t]\n\ts_cbranch_scc0 0b\n\t"
               "s_cmp_eq_u64 s[64:65], %[t]\n\ts_cbranch_scc0 0b\n\t"
               "s_cmp_eq_u64 s[66:67], %[t]\n\ts_cbranch_scc0 0b\n\t"
               "s_cmp_eq_u64 s[68:69], %[t]\n\ts_cbranch_scc0 0b\n\t"
               "s_cmp_eq_u64 s[70:71], %[t]\n\ts_cbranch_scc0 0b\n\t"
               "s_cmp_eq_u64 s[72:73], %[t]\n\ts_cbranch_scc0 0b\n\t"
               "s_cmp_eq_u64 s[74:75], %[t]\n\ts_cbranch_scc0 0b\n\t"
               "s_cmp_eq_u64 s[76:77], %[t]\n\ts_cbranch_scc0 0b\n\t"
               "s_cmp_eq_u64 s[78:79], %[t]\n\ts_cbranch_scc0 0b\n\t"
               "s_cmp_eq_u64 s[80:81], %[t]\n\ts_cbranch_scc0 0b\n\t"
               "s_cmp_eq_u64 s[82:83], %[t]\n\ts_cbranch_scc0 0b\n\t"
               "s_cmp_eq_u64 s[84:85], %[t]\n\ts_cbranch_scc0 0b\n\t"
               "s_cmp_eq_u64 s[86:87], %[t]\n\ts_cbranch_scc0 0b\n\t"
               "s_cmp_eq_u64 s[88:89], %[t]\n\ts_cbranch_scc0 0b\n\t"
               "s_cmp_eq_u64 s[90:91], %[t]\n\ts_cbranch_scc0 0b\n\t"
               "s_cmp_eq_u64 s[92:93], %[t]\n\ts_cbranch_scc0 0b\n\t"
               "s_cmp_eq_u64 s[94:95], %[t]\n\ts_cbranch_scc0 0b\n\t"
               "s_cmp_eq_u64 s[96:97], %[t]\n\ts_cbranch_scc0 0b\n\t"
               "s_cmp_eq_u64 s[98:99], %[t]\n\ts_cbranch_scc0 0b\n\t"
               "\n1:" : [n] "=&s"(n) : [p] "s"(ws), [t] "s"(tag | (unsigned long long)tag << 32), [m] "i"(POLL_MAX * 16) : "memory", "scc", "s52", "s53", "s54", "s55", "s56", "s57", "s58", "s59", "s60", "s61", "s62", "s63", "s64", "s65", "s66", "s67", "s68", "s69", "s70", "s71", "s72", "s73", "s74", "s75", "s76", "s77", "s78", "s79", "s80", "s81", "s82", "s83", "s84", "s85", "s86", "s87", "s88", "s89", "s90", "s91", "s92", "s93", "s94", "s95", "s96", "s97", "s98", "s99"); }
#define NT "nt "
DEV void dma1(rsrc_t r, u32 vo, u32 so, u32 ld) { asm volatile("s_mov_b32 m0, %3\n\ts_nop 0\n\tbuffer_load_dwordx4 %0, %1, %2 offen " NT "lds" :: "v"(vo), "s"(r), "s"(so), "s"(ld) : "memory"); }
template <u32 B, u32 E, u32 P> DEV void pull(rsrc_t r, u32 so, u32 part, u32 l16) {
  for (u32 i = B; i < E; i += P * 8192) asm volatile("s_mov_b32 m0, %3\n\ts_nop 0\n\tbuffer_load_dword %0, %1, %2 offen sc1 lds" :: "v"(min(i + l16 * (P * 8) + part * 128, E - 128)), "s"(r), "s"(so), "i"(L_ATT) : "memory"); }
DEV float csum(u32x2 lo, u32x2 hi, float c) { return dot2(lo[0], 0x3f803f80u, dot2(lo[1], 0x3f803f80u, dot2(hi[0], 0x3f803f80u, dot2(hi[1], 0x3f803f80u, c)))); }
DEV void st4(float v, rsrc_t r, u32 vo, u32 so) { __builtin_amdgcn_raw_buffer_store_b32(asu(v), r, vo, so, SAUX); }
DEV float ld4(rsrc_t r, u32 vo, u32 so) { return asf(__builtin_amdgcn_raw_buffer_load_b32(r, vo, so, 16)); }
typedef const unsigned short* cw_t;

__global__ void __launch_bounds__(512, 2) mega(cw_t wo, cw_t wqkv, cw_t xin, cw_t wg, cw_t wu, cw_t wd, unsigned short* __restrict__ out, unsigned char* __restrict__ wsp) {
  GAS unsigned char* ws = (GAS unsigned char*)wsp;
  const int w0 = __builtin_amdgcn_readfirstlane(threadIdx.x >> 6);
#define PRB(k) do {} while (0)
  unsigned long long src; u32 junk = 0;
  asm volatile("s_load_dwordx2 %0, %1, %2" : "=s"(src) : "s"(__builtin_amdgcn_kernarg_segment_ptr()), "s"(max(max(w0 >> 1, 1), w0 - 3) * 8));
  (void)wqkv; (void)xin; (void)wg; (void)wu;
  const int bx = blockIdx.x & 7, br = blockIdx.x >> 3;
  if (threadIdx.x - 192 < 15) { unsigned long long pc0;
    asm volatile("s_getpc_b64 %1\n\tglobal_load_dword %0, %2, %1" : "=v"(junk), "=&s"(pc0) : "v"((threadIdx.x - 192) * 4096 + br * 128) : "memory"); }
  const bool isC = w0 < 4;
  u32 pk = ((bx & 3) * 4 + (br & 3)) | (((bx >> 2) * 8 + (br >> 2)) << 4) | (w0 << 8);
  {
    const u32 rb = pk & 15, s = pk >> 4 & 15, lane = threadIdx.x & 63;
    u32 M, pa, last, R, e, t, i, pc, x; u32x4 r;
    asm volatile("s_movk_i32 %[M], 4682\n\ts_movk_i32 %[pa], 3232\n\ts_movk_i32 %[la], 559\n\ts_movk_i32 %[R], 540\n\t"
                 "s_cmp_lt_u32 %[w], 6\n\ts_cmovk_i32 %[M], 6554\n\ts_cmovk_i32 %[pa], 1120\n\ts_cmovk_i32 %[la], 319\n\ts_cmovk_i32 %[R], 640\n\ts_cselect_b32 %[e], %[ex], %[eg]\n\t"
                 "s_cmp_lt_u32 %[w], 4\n\ts_cmovk_i32 %[M], 13108\n\ts_cmovk_i32 %[pa], 3760\n\ts_cmovk_i32 %[la], 199\n\ts_cmovk_i32 %[R], 600\n\ts_cselect_b32 %[e], %[eq], %[e]"
                 : [M] "=&s"(M), [pa] "=&s"(pa), [la] "=&s"(last), [R] "=&s"(R), [e] "=&s"(e)
                 : [w] "s"(w0), [eq] "s"(s * 80 + ((w0 + 1) & (u32)(w0 - 2) >> 8) * 1280), [ex] "s"(((s & 1) * 2 + (w0 & 1)) * 40960 + (s >> 1) * 160), [eg] "s"((s * 7 - (s >> 3) * 2 - (((s & 7) + 1) >> 3)) * 32) : "scc");
    e += rb * R * 256; R = w0 == 3 ? 0 : R * 4096; t = (w0 + (EB && w0 >= 6 ? 5 : 0)) * UN;
    OPQS(e); OPQS(R); OPQS(t);
    asm volatile("s_waitcnt lgkmcnt(0)" : "+s"(src));
    if (GSLP && w0 >= 6) __builtin_amdgcn_s_sleep(GSLP);
    r = u32x4{(u32)src, (u32)(src >> 32) & 0xffffu, R, 0x00020000u};
    asm volatile("s_mov_b32 m0, %[t]\n\ts_mov_b32 %[i], 0\n0:\n\tv_add_u32 %[pc], %[i], %[ln]\n\tv_min_u32 %[pc], %[la], %[pc]\n\tv_mul_u32_u24 %[x], %[M], %[pc]\n\tv_lshrrev_b32 %[x], 16, %[x]\n\t"
                 "v_mul_u32_u24 %[x], %[pa], %[x]\n\tv_lshl_add_u32 %[x], %[pc], 4, %[x]\n\ts_add_u32 %[i], %[i], 64\n\tbuffer_load_dwordx4 %[x], %[r], %[e] offen " NT "lds\n\t"
                 "s_add_u32 m0, m0, 0x400\n\ts_cmp_le_u32 %[i], %[la]\n\ts_cbranch_scc1 0b"
                 : [i] "=&s"(i), [pc] "=&v"(pc), [x] "=&v"(x) : [t] "s"(t), [ln] "v"(lane), [la] "s"(last), [M] "s"(M), [pa] "s"(pa), [r] "s"(r), [e] "s"(e) : "memory", "scc");
    PIN();
  }
  const rsrc_t rs = __builtin_amdgcn_make_buffer_rsrc((void*)wsp, 0, OFF_END, 0x00020000);
  u32 tag;
#define MBINIT() do { lds_post(c.mb + (isC ? 1024 : 2048), isC ? 0 : EB && w >= 6 ? -1 : (SPOLL & 8) && w == 6 ? 8 : 16); \
    if (!isC) { lds_post(c.mb, g < 2 ? 0 : T_KV - 1); lds_post(L_QF + lane * 4, 0); } } while (0)
#define XE_A ((g >> 1) * 1024 + (l15 + 32 * (g & 1)) * 16)
#define XE_C (4096 + (l15 + 32 * (g >> 1)) * 16)
#define STR_INIT(first) Str t; t.rs = rs; t.l16 = c.l16; t.fa = L_FREE + (lane & 4) * 256 + MBW(lane) + (lane & 7) * 4; t.fl = L_FULL + wq * (XF ? 320 : 256); t.seq = OFF_FLAG + (rb * 16 + s) * 8192 + 4096 + wq * 1024 + (fb + (first)) * 4; t.hl = wq * 5120;
  {
    LANES();
    const int q = l15 >> 2, p = l15 & 3;
    const u32 lb = (w + (EB && w >= 6 ? 5 : 0)) * UN, T = rb * 4 + half * 2 + wq, up = w & 1;
    const u32 cso = OFF_CS + (s * 16 + rb) * 2048, slot = OFF_SLOT + (rb * 16 + s) * 1024;
    u32x4 zero = {0u, 0u, 0u, 0u}; OPQV(zero);
    if (w < 6) {
      MBINIT();
      tag = call_tag(wsp);
      if (w < 3) {
        if (lane < 16) st16(zero, rs, (lane >> 3) * 2048 + (lane & 7) * 16, w ? OFF_END : fl + F_KV * 128);
        const u32 dso = OFF_WQ + s * 163840, fm = w < 2 ? 2048 : 0, fo = dso + (w < 2 ? w * 1024 : 120 * 1024);
        u32 da[4];
        for (int i = 0; i < 4; ++i) { const u32 hs = rb * 5 + min(i < 3 ? 2 * i + hh : lane >> 3, 4), ks = hs >> 1;
          da[i] = hs * 512 + min(ks, (u32)K1) * (i < 3 ? fm : 2048) + (ks < K1 ? 0 : (i < 3 ? w & 1 : 2) * (39 - K1) * 1024) + (i < 3 ? l31 : lane & 7) * 16; OPQV(da[i]); }
        WAITV(1); PST(1);
        u32x2 lo[4], hi[4];
        for (int i = 0; i < 4; ++i) { const u32 a = lb + q * 80 + (i < 3 ? min(2 * i + hh, 4) * 640 + (g & 1) * 40 + p * 8 : min(2 * g + (p >> 1), 4) * 640 + (p & 1) * 40 + 32);
          lo[i] = lds_tr(a); hi[i] = lds_tr(a + 320); }
        for (int i = 0; i < 4; ++i) st16(u32x4{lo[i][0], lo[i][1], hi[i][0], hi[i][1]}, rs, da[i], i < 3 ? fo : dso + 2048 + (w ? 3 - w : 0) * 128);
        float cm = csum(lo[0], hi[0], csum(lo[1], hi[1], hh ? 0.f : csum(lo[2], hi[2], 0.f))), cr = lane < 40 ? csum(lo[3], hi[3], 0.f) : 0.f;
        cr += asf(__builtin_amdgcn_mov_dpp(asu(cr), 0x128, 0xf, 0xf, true));
        cr = swap32_add(swap16_add(cr)); cm = swap32_add(cm);
        st4(hh ? cr : cm, rs, lane * 4, cso + w * 256);
        PST(2);
        WAITV(0); PST(3);
        if (lane < 16) put_tag(ws, OFF_SLOT + s * 1024 + lane * 16384 + (w * 16 + rb) * 4, tag);
      } else if (w != 3) {
        st16(u32x4{4u * lane, 4u * lane + 1, 4u * lane + 2, 4u * lane + 3}, rs, c.l16, fl + 4096 + wq * 1024);
        st16(zero, rs, c.l16 + 1024, fl + wq * 2048);
        if ((lane >> 3) != F_KV) st16(zero, rs, c.l16, fl + wq * 2048);
        WAITV(3); PST(1);
        float sm = 0, sq = 0;
        for (int k = 0; k < 5; ++k) { const u32x4 xv = lds_ld<u32x4>(lb + l31 * 160 + hh * 16 + k * 32);
          st16(xv, rs, c.l16 + k * 1024, OFF_X + T * XT + cs * 5120);
          for (int j = 0; j < 4; ++j) { sm = dot2(xv[j], 0x3f803f80u, sm); sq = dot2(xv[j], xv[j], sq); } }
        sm = swap32_add(sm); sq = swap32_add(sq);
        if (!hh) __builtin_amdgcn_raw_buffer_store_b64(u32x2{asu(sm), asu(sq)}, rs, l31 * 8, OFF_X + T * XT + 40960 + cs * 256, SAUX);
        PST(2);
        WAITV(0); PST(3);
        if (lane < 16) put_tag(ws, OFF_SLOT + rb * 16384 + lane * 1024 + (64 + (T & 3) * 8 + cs) * 4, tag);
      }
      BAR();
      PST(4);
      if (SPOLL & 4) { if (isC) tag8(ws, slot + 256 + wq * 32, tag); else tag48(ws, slot, tag); }
      else poll_tag(ws, isC ? slot + 256 + wq * 32 + (lane & 3) * 8 : slot + (lane < 24 ? lane : 0) * 8, tag);
      PST(6);
      asm volatile("" :: "v"(junk));
      if (!isC) { STR_INIT(0); stream<NA>(t, OFF_WQ + s * 163840, 0, EB ? 11 : 15, -100, 0);
        if (EB) stream<NA>(t, OFF_WQ + s * 163840 + 11 * UN, 11, 4, 0, 0); }
    } else {
      const u32 z = (rb * 2 + half) * 2 + up;
      u32 dso[2];
      for (int n = 0; n < 2; ++n) { const u32 u = z + 64 * n;
        const bool isO = n == 0 && u < 20;
        const int v = isO ? u : u - 20, kh = v >= (isO ? 10 : 27), st = v - kh * (isO ? 10 : 27);
        const rsrc_t r = __builtin_amdgcn_make_buffer_rsrc((void*)(isO ? wo : wd), 0, isO ? 640 * 1280 : 1728 * 1280, 0x00020000);
        const bool rem = isO && st >= 8;
        const u32 s7 = (st & 3) * 2, r0 = isO ? (kh * 8 + st + (rem ? st * 3 - 32 : 0)) * 40 + (rem ? 16 : 0) : (kh * 54 + 7 * s7 + (st >> 2)) * 16;
        const u32 m4 = isO ? (rem ? 80 : 20) : s7 < 6 ? 112 : 96, m3 = rem ? 40 : 4, m2 = rem ? 20 : 8;
        dso[n] = u < 74 ? (isO ? OFF_WO + cs * 102400 : OFF_WD + cs * 276480) + (isO && !rem ? (st - ROT * cs) & 7 : st) * UN + kh * 5120 : OFF_END;
        if (u < 74) {
#pragma unroll 1
          for (u32 i = 0; i < 320; i += 64) { const u32 pc = i + lane, sl = (pc & 511) * 6554 >> 16;
            dma1(r, ((sl >> 4) * m4 + (sl >> 3 & 1) * m3 + (sl >> 2 & 1) * m2 + (sl & 3)) * 1280 + (pc - sl * 10) * 16, r0 * 1280 + cs * 160, lb + 2 * UN + n * 5120 + i * 16); } } }
      MBINIT();
      if (EB) BAR();
      if (EB & 2) __builtin_amdgcn_s_setprio(3);
      tag = call_tag(wsp);
      WAITV(0); PST(1);
      u32x4 y[20];
      for (int i = 0; i < 20; ++i) { const int jn = i % 5;
        const u32 a = i < 10 ? lb + 2 * UN + i / 5 * 5120 + (8 * g + q) * 160 + (jn < 4 ? (jn >> 1) * 32 + 8 * p + 4 * (jn & 1) : 64 + 4 * p) * 2 : lb + (8 * jn + q) * 224 + (g + i / 15 * 4) * 32 + p * 8;
        const u32x2 l = lds_tr(a), h = lds_tr(a + (i < 10 ? 640 : 896)); y[i] = u32x4{l[0], l[1], h[0], h[1]}; }
      if (EB) { for (int i = 0; i < 20; ++i) OPQV(y[i]);
        asm volatile("s_waitcnt lgkmcnt(0)" ::: "memory"); lds_post(c.mb + 2048, !(EB & 4) && (SPOLL & 8) && w == 6 ? 8 : 16); }
      else BAR();
      PST(4);
      if (DEFER & 1) { if (SPOLL & 4) tag48(ws, slot, tag); else poll_tag(ws, slot + (lane < 24 ? lane : 0) * 8, tag); }
      if (DEFER >> 1) __builtin_amdgcn_s_sleep(DEFER >> 1);
      PST(6);
      auto cs0 = [&]() {
        u32x4 d; float cv[32];
        for (int j = 0; j < 4; ++j) {
          const u32 vo = (j == 2 ? (l31 < 8 ? 0 : l31 < 16 ? 512 : 256) + 128 + (lane & 7) * 4 : (j < 2 ? j : 2) * 256 + l31 * 4) + hh * 2048;
          for (int i = 0; i < 8; ++i) cv[j * 8 + i] = ld4(rs, vo, OFF_CS + s * 32768 + i * 4096); }
        PIN();
        for (int j = 0; j < 4; ++j) { float v = 0;
          for (int i = 0; i < 8; ++i) v += cv[j * 8 + i];
          const u32 pp = split2(swap32_add(v)), x = hh ? pp >> 16 : pp & 0xffffu; d[j] = x | (x << 16); }
        lds_st<u32x4>(L_CS + c.l16, d);
        lds_post(L_FULL + MBW(g) + l15 * 4, T_EV(0) - 1); };
      if ((DEFER & 1) && w == 6) cs0();
      for (int i = 0; i < 10; ++i) st16(y[i], rs, c.l16 + i % 5 * 1024, dso[i / 5]);
      for (int j = 0; j < 2; ++j) { const int t = g + 4 * j; float cm = 0;
        for (int h = 0; h < 5; ++h) { const int hs = rb * 5 + h, ks = hs >> 1; const u32x4 v = y[10 + j * 5 + h];
          const u32 fm = ks < K1 ? 1024 : (40 - K1) * 1024, fa = (ks < K1 ? ks * 2 : 2 * K1) * 1024;
          const u32 f = j ? (t < 7 ? t * 40960 : OFF_END) : t < 3 ? t * fm + fa : 3 * 40960;
          st16(v, rs, f + l15 * 16, OFF_WG + s * 286720 + ks * 1024 + (hs & 1) * 512 + up * 256);
          cm = csum(u32x2{v[0], v[1]}, u32x2{v[2], v[3]}, cm); }
        st4(cm, rs, lane * 4, cso + 768 + up * 512 + j * 256); }
      PST(2);
      WAITV(0); PST(3);
      if (lane < 32) put_tag(ws, OFF_SLOT + l15 * 16384 + (cs * 2 + g) * 1024 + (128 + z) * 4, tag);
      if (!(DEFER & 1) && w == 6) { if (SPOLL & 4) tag48(ws, slot, tag); else poll_tag(ws, slot + (lane < 24 ? lane : 0) * 8, tag); cs0(); }
      if (!(EB & 4) && (SPOLL & 8) && w == 6) { tag32(ws, slot + 512, tag); tag32(ws, slot + 640, tag); lds_post(c.mb + 2048, 16); }
      if (EB & 2) __builtin_amdgcn_s_setprio(0);
    }
    PST(7);
  }

#pragma unroll 1
  for (; pk < 8192; pk += 4096) {
    u32x4 qm, Km[9]; u32x2 qr, Kr[9], Vt[9][4];
    At at;
    auto ldk = [&](auto ii) { constexpr int i = decltype(ii)::v; Kr[i] = lds_ld<u32x2>(at.aR + (8 - i) * KVL); PIN(); Km[i] = lds_ld<u32x4>(at.aK + (8 - i) * KVL); };
    auto ldv = [&](auto ii) { constexpr int i = decltype(ii)::v; for (int r = 0; r < 4; ++r) Vt[i][r] = lds_tr(at.aV[r] + (8 - i) * KVL); };
    f32x16 cm, s0;
    auto first = [&]() { for (int r = 0; r < 16; ++r) cm[r] = at.cm[r];
      OPQV(cm); s0 = mm32k8p(Kr[0], qr, cm); };
    auto kvpair = [&]() { SCAL();
      const u32 j = 3 + hd * 10 - w;
      const u32 src = rb * 4 + j >= 8 ? kvb + (rb * 4 - 8 + j) * KVT : OFF_END + 2 * KVT;
      const u32 one = fl - layer * 2048 + 4100, ld = L_KV + j * KVL, fo = fl + F_KV * 128, pre = rb >= 2 ? 0 : 0xff - rb * 15;
      u32 v = pre;
      kvtile(ws, fo, v, pre, j ^ 4, rs, at.l16, src, ld, one);
      kvtile(ws, fo, v, pre, (j ^ 4) - 2, rs, at.l16, src - 2 * KVT, ld - 2 * KVL, one); };
    if (pk < 4096) ST(0);
    if (isC) {
      LANES();
      __builtin_amdgcn_s_setprio(CPRIO);
      u32 xso = OFF_X + layer * 2 * XSZ + (rb * 4 + wq) * XT, kso = kvb + (rb * 4 + wq) * KVT; GAS unsigned char* sg = ws + fl + F_KV * 128; OPQS(xso); OPQS(kso); OPQS(sg);
      if (layer) poll_ge<0>(ws, fl + (F_X + wq) * 128, 16);
      STQ(0); LQA(0);
      if (!layer) PRB(4); else PRB(5);
      u32x4 A[40], W[160];
      sfor<0, ALA>([&](auto i) { constexpr int k = decltype(i)::v; A[k] = ld16(rs, c.l16 + k % 4 * 1024, xso + k / 4 * 4096); PIN(); });
      Stat sa;
      for (int i = 0; i < 4; ++i) sa.sp[i] = ld8(rs, hh * 1024 + l31 * 8 + i * 256, xso + 40960);
      spin_ge<0>(c.mb, fb + 1);
      f32x16 acc[3], aq;
      const f32x16 z16 = {};
      float& rstd = sa.rstd; u32& mo = sa.mo; float r2; int fv; u32x4 cq, t0, t3; u64 fm;
      auto ahead = [&](auto ni) { constexpr int n = decltype(ni)::v;
        if constexpr (n < 160) {
          if constexpr (n % 10 == 0 && n) chk(fm, c.mb, fb + n / 10 + 1);
          if constexpr (n % 10 == 9 && n < 150) fm = pre(fv, fb + n / 10 + 2);
          W[n] = far_ld<(n % 150) * 1024, u32x4>(c);
          if constexpr (n % 10 == 9) freed(c);
          if constexpr (n % 10 == 4 && n < 150) fv = lds_word(c.mb);
        } };
      sfor<0, PD>(ahead);
      PIN();
      u32 kp[22], ta, qf, qv;
      const float qsc = 0.22360679774997896f * 1.4426950408889634f;
      sfor<0, 160>([&](auto fi) {
        constexpr int f = decltype(fi)::v, t = gu_tile(f), ks = gu_ks(f), n = f + PD;
        if constexpr (f >= 120) aq = mm32p(W[f], A[ks], aq);
        else if constexpr (ks == 0) acc[t] = mm32p(W[f], A[ks], z16); else acc[t] = mm32p(W[f], A[ks], acc[t]);
        ahead(IC<n>{});
        if constexpr (ag_at(f) >= 0) { constexpr int k = ag_at(f); OPQV(c.l16); A[k] = ld16(rs, c.l16 + k % 4 * 1024, xso + k / 4 * 4096); }
        if constexpr (st_at(f) >= 0) stat<st_at(f)>(sa);
        if constexpr (f == 64) { r2 = rstd * qsc; ta = L_KV + wq * KVL + c.l16; OPQV(r2); OPQV(ta); }
        if constexpr (f == 96) cq = far_ld<L_CS, u32x4>(c);
        if constexpr (f == 97) t0 = lds_ld<u32x4>(ta);
        if constexpr (f == 100) acc[0] = mm32k8p(u32x2{cq[0], cq[1]}, u32x2{mo, 0u}, acc[0]);
        if constexpr (f == 101) acc[2] = mm32k8p(u32x2{cq[2], cq[3]}, u32x2{mo, 0u}, acc[2]);
        if constexpr (f == 107) acc[1] = mm32k8p(u32x2{cq[0], cq[1]}, u32x2{0u, mo}, acc[1]);
        if constexpr (f == 118) aq = mm32k8p(u32x2{cq[2], cq[3]}, u32x2{0u, mo}, z16);
        if constexpr (f >= 102 && f < 118) { constexpr int j = f - 102; kp[j] = mulpk(acc[j / 8][j % 8 * 2], acc[j / 8][j % 8 * 2 + 1], rstd); }
        if constexpr (f >= 106 && f < 120 && (f - 106) % 4 < 2) {
          constexpr int q = (f - 106) / 4;
          const u32x4 v = {kp[4 * q], kp[4 * q + 1], kp[4 * q + 2], kp[4 * q + 3]};
          if constexpr ((f - 106) % 4 == 0) __builtin_amdgcn_raw_buffer_store_b128(v, rs, (q < 2 ? c.l16 : t0[0]) + q * 1024, kso, SAUX);
          else lds_st<u32x4>((q < 2 ? t0[1] : t0[2]) + q * 1024, v);
        }
        if constexpr (f == 121) { asm volatile("" :: "v"(acc[2])); kp[16] = mulpk(acc[2][0], acc[2][1], rstd); kp[17] = mulpk(acc[2][2], acc[2][3], rstd); }
        if constexpr (f == 122) { kp[20] = mulpk(acc[2][8], acc[2][9], rstd); kp[21] = mulpk(acc[2][10], acc[2][11], rstd);
          __builtin_amdgcn_raw_buffer_store_b128(u32x4{kp[16], kp[17], kp[20], kp[21]}, rs, t0[0] + 3072, kso + 1024, SAUX); }
        if constexpr (f == 123) { lds_st<u32x4>(t0[2] + 4224, u32x4{kp[16], kp[17], kp[20], kp[21]}); kp[18] = mulpk(acc[2][4], acc[2][5], r2); }
        if constexpr (f == 124) kp[19] = mulpk(acc[2][6], acc[2][7], r2);
        if constexpr (f == 127) lds_st<u32x2>(t0[3], u32x2{kp[18], kp[19]});
        if constexpr (f == 128) t3 = lds_ld<u32x4>(ta + 3072);
        if constexpr (f == 129) { const u32x4 x = lds_ld<u32x4>(ta + 1024); at.aK = x[0]; at.aR = x[1]; at.o0 = x[2]; at.o1 = x[3]; }
        if constexpr (f == 130) { const u32x4 x = lds_ld<u32x4>(ta + 2048); for (int r = 0; r < 4; ++r) at.aV[r] = x[r]; }
        if constexpr (f >= 131 && f < 135) { const f32x4 x = lds_ld<f32x4>(c.l16 + L_KV + (f - 131) * KVL + 4224); for (int r = 0; r < 4; ++r) at.cm[(f - 131) * 4 + r] = x[r]; }
        if constexpr (f == 148) { at.l16 = c.l16; at.mb = c.mb + 1024; qf = L_QF + wq * 64; qv = layer + 1; OPQV(at.mb); OPQV(qf); OPQV(qv); }
        if constexpr (f == 149) { qr = c.l16 >= 512 ? u32x2{0u, 0u} : u32x2{kp[18], kp[19]}; OPQV(qr); }
        if constexpr (f == 153) fv = lds_word(t3[2]);
        if constexpr (f == 158) fm = pre(fv, eb + 14);
        if constexpr (f == 159) { chk(fm, t3[2], eb + 14); ldk(IC<1>{}); }
        if constexpr (f == 156) ldk(IC<0>{});
        if constexpr (f == 157) ldv(IC<0>{});
        if constexpr (f == 0) { STQ(1); LQA(1); }
        if constexpr (f == 60) { STQ(2); LQA(2); }
        if constexpr (qd_at(f) >= 0) STQ(qd_at(f));
        if constexpr (f == 120) STQ(3);
        PIN();
        if constexpr (f == KSIG) {
          sigx<3>(sg, t3[0], t3[1]);
          PIN();
        }
      });
      STQ(4);
      if (!layer) PRB(6);
      {
        first();
        PIN();
        lds_st<u32x4>(t3[3], u32x4{pk2(aq[8] * r2, aq[9] * r2), pk2(aq[10] * r2, aq[11] * r2), pk2(aq[12] * r2, aq[13] * r2), pk2(aq[14] * r2, aq[15] * r2)});
        lds_post(qf, qv);
        PIN();
        qm = u32x4{pk2(aq[0] * r2, aq[1] * r2), pk2(aq[2] * r2, aq[3] * r2), pk2(aq[4] * r2, aq[5] * r2), pk2(aq[6] * r2, aq[7] * r2)};
        OPQV(qm);
        __builtin_amdgcn_s_setprio(0);
      }
    } else {
      LANES();
      sfor<0, NSET>([&](auto k) { aset<decltype(k)::v>(at, c, 3 - wq, 1, s & 7); });
      if (wq < 2) {
        STR_INIT(15);
        stream<57>(t, OFF_WQ + s * 163840 + 15 * UN, 0, 1, eb + 1, 0);
        if (!(SPOLL & 8) && !layer) poll_tag(ws, OFF_SLOT + (rb * 16 + s) * 1024 + 512 + l31 * 8, tag);
        ST(1);
        if (!(EB & 4) || layer) stream<57>(t, OFF_WO + cs * 102400, 8, 6, eb + 9, 1);
      } else if (wq == 2) {
        if (PFM & 16) pull<0, 10 * UN, 8>(rs, OFF_WO + cs * 102400, (rb & 3) * 2 + half, c.l16);
      } else if (wq == 3) {
        At a0; u32x4 tb[16];
        sfor<9, NSET>([&](auto k) { aset<decltype(k)::v>(a0, c, 0, 0, s & 7); });
        const u32 fa = L_FREE + MBW(lane) + (lane & 3) * 4, vo = (2 * l31 + hh) * 16, kq = c.l16 + 16 < (16 - rb) * 16 ? c.l16 * 8192 + 131072 : 64;
        for (u32 i = 0; i < 4; ++i) {
          tb[4 * i] = u32x4{vo, L_KV + (8 + i) * KVL + c.l16, L_KV + (8 + i) * KVL + vo, L_QH + i * 1536 + 1024 + lane * 8};
          tb[4 * i + 1] = u32x4{a0.aK + i * KVL, a0.aR + i * KVL, a0.o0, a0.o1};
          tb[4 * i + 2] = u32x4{a0.aV[0] + i * KVL, a0.aV[1] + i * KVL, a0.aV[2] + i * KVL, a0.aV[3] + i * KVL};
          tb[4 * i + 3] = u32x4{kq, max(c.l16, 1u) << i, fa, L_QH + i * 1536 + c.l16};
          for (int k = 0; k < 4; ++k) OPQV(tb[4 * i + k]); }
        u32 one = ((int)(7 - rb * 4 - lane) >> 31) & 0x3f803f80u, oa = L_KV + min(lane, 12) * KVL + 4096; OPQV(one); OPQV(oa);
        spin_ge<1>(fa, eb + 8);
        lds_st<u32x2>(oa, u32x2{one, (u32)lane >> 3});
        for (int i = 0; i < 4; ++i) lds_st<u32x4>(L_KV + i * KVL + c.l16, tb[4 * i]);
        lds_post(L_FULL + MBW(g) + 64 + l15 * 4, fb + T_TB - 1);
        PIN();
        for (int i = 0; i < 4; ++i) { for (int k = 1; k < 4; ++k) lds_st<u32x4>(L_KV + i * KVL + k * 1024 + c.l16, tb[4 * i + k]);
          lds_st<f32x4>(L_KV + i * KVL + 4224 + c.l16, f32x4{at.cm[4 * i], at.cm[4 * i + 1], at.cm[4 * i + 2], at.cm[4 * i + 3]}); }
        PIN();
        lds_post(L_FULL + MBW(g) + 64 + l15 * 4, fb + T_CS - 1);
      }
      spin_ge<1>(L_FREE + MBW(lane) + (lane & 3) * 4, eb + 14);
      ldk(IC<0>{}); ldv(IC<0>{});
      u32 qa = L_QH + (3 - wq) * 1536 + c.l16;
      qr = lds_ld<u32x2>(L_QH + (3 - wq) * 1536 + 1280 + l31 * 8);
      if (hh) qr = u32x2{0u, 0u};
      OPQV(qa);
      first();
      ldk(IC<1>{});
      if (wq >= 2) {
        kvpair();
        if ((PFM & 1) && wq == 2) pull<0, 15 * UN, 4>(rs, OFF_WG + s * 286720, rb & 3, c.l16);
        __builtin_amdgcn_s_setprio(APRIO);
        STQ(9);
      } else {
        STR_INIT(22);
        if (!(EB & 4) || layer) stream<57>(t, OFF_WO + cs * 102400 + 6 * UN, 14, 1, eb + 15, 1);
        __builtin_amdgcn_s_setprio(BPRIO);
        ST(2);
      }
      { u32 t, n;
        asm volatile("s_mov_b32 %[n], %[m]\n0:\n\tds_read_b32 %[t], %[f]\n\tds_read_b128 %[qm], %[a]\n\ts_sub_u32 %[n], %[n], 1\n\ts_waitcnt lgkmcnt(0)\n\ts_cbranch_scc1 1f\n\tv_cmp_ge_u32 vcc, %[l], %[t]\n\ts_cbranch_vccnz 0b\n1:"
                     : [t] "=&v"(t), [n] "=&s"(n), [qm] "=&v"(qm) : [f] "v"(L_QF + (3 - wq) * 64), [a] "v"(qa), [l] "s"(layer), [m] "i"(SPIN_MAX) : "memory", "scc", "vcc"); }
    }
    {
      SCAL();
      const u32 qt = hd ? 3 - wq : wq;
      const bool hh = at.l16 >= 512;
      STQ(5);
      u32 oso, inc, mark; u32x4 p1;
      const f32x16 z16 = {};
      f32x16 sc[9], o;
      sc[0] = mm32p(Km[0], qm, s0);
      PIN();
      for (int r = 0; r < 8; ++r) cm[r] = asf(asu(cm[r]) ^ 0xff800000u);
      OPQV(cm);
      sc[1] = mm32p(Km[1], qm, z16);
      u32 lo; asm volatile("v_bfrev_b32 %0, 1" : "=v"(lo));
      PIN();
      {
        auto mx = [](float x, float y, float z) { return fmaxf(fmaxf(x, y), z); };
        const f32x16& x = sc[0];
        const float m = mx(mx(mx(x[0], x[1], x[2]), x[9], x[10]), mx(mx(mx(x[3], x[4], x[5]), x[11], x[12]), x[15], x[15]), mx(mx(x[6], x[7], x[8]), x[13], x[14]));
        auto r = __builtin_amdgcn_permlane32_swap(lo, asu(m), false, false);
        u32 sh; asm volatile("v_max_f32 %0, %1, %2\n\tv_cvt_pk_bf16_f32 %0, -%0, 0\n\ts_nop 0" : "=v"(sh) : "v"(r[0]), "v"(r[1]));
        sc[0] = mm32k8p(Kr[0], u32x2{sh, 0u}, sc[0]);
        qr[0] |= sh;
        PIN();
        for (int r = 8; r < 16; ++r) cm[r] = asf(asu(cm[r]) ^ 0xff800000u);
        OPQV(cm);
      }
      sfor<0, 9>([&](auto ii) {
        constexpr int i = decltype(ii)::v;
        float e[16];
        auto ex = [&](int a, int b) { for (int r = a; r < b; ++r) e[r] = __builtin_amdgcn_exp2f(sc[i][r]); };
        auto vv = [&](int t, int h) { return u32x4{Vt[t][2 * h][0], Vt[t][2 * h][1], Vt[t][2 * h + 1][0], Vt[t][2 * h + 1][1]}; };
        if constexpr (i >= 1) { asm volatile("" :: "v"(Vt[i][3])); PIN(); }
        if constexpr (i >= 1 && i < 8) sc[i + 1] = mm32p(Km[i + 1], qm, i < 7 ? z16 : cm);
        PIN();
        if constexpr (i < 7) ldk(IC<i + 2>{});
        ex(0, 4);
        PIN();
        if constexpr (i >= 1) o = mm32p(vv(i - 1, 1), p1, o);
        PIN();
        if constexpr (i < 8) for (int r = 0; r < 2; ++r) Vt[i + 1][r] = lds_tr(at.aV[r] + (7 - i) * KVL);
        ex(4, 8);
        PIN();
        if constexpr (i < 8) sc[i + 1] = mm32k8p(Kr[i + 1], qr, sc[i + 1]);
        PIN();
        const u32x4 p0 = {pk2(e[0], e[1]), pk2(e[2], e[3]), pk2(e[4], e[5]), pk2(e[6], e[7])};
        if constexpr (i < 8) for (int r = 2; r < 4; ++r) Vt[i + 1][r] = lds_tr(at.aV[r] + (7 - i) * KVL);
        if constexpr (i < 8) asm volatile("v_readlane_b32 %0, %1, 32" : "=s"(mark) : "v"(Kr[i + 1][1]));
        PIN();
        ex(8, 11);
        OPQV(e[8]); OPQV(e[9]); OPQV(e[10]);
        PIN();
        o = mm32p(vv(i, 0), p0, i ? o : z16);
        PIN();
        if constexpr (i < 2) if (__builtin_expect(w == i, 0)) kvpair();
        if constexpr (i < 8) if (__builtin_expect(mark == 0, 0)) redo<i + 1>(sc[i + 1], Vt[i + 1], Km[i < 7 ? i + 2 : 8], Kr[i < 7 ? i + 2 : 8], at, qm, qr, cm);
        ex(11, 16);
        p1 = u32x4{pk2(e[8], e[9]), pk2(e[10], e[11]), pk2(e[12], e[13]), pk2(e[14], e[15])};
        OPQV(p1);
        if constexpr (i == 8) o = mm32p(vv(8, 1), p1, o);
        PIN();
        if constexpr (i == 4) if (!isC) STQ(12);
        if constexpr (i == 8) { oso = OFF_O + layer * OSZ + (rb * 8 + qt * 2) * 20480 + (s >> 3) * 10240; inc = 1 + hd * (LF - 1); OPQS(oso); OPQV(inc); PIN(); }
        if constexpr (i == 0) {
          if (!isC) STQ(11);
          PIN();
        }
      });
      u32x4 om, orr;
      auto fin = [&]() { const float il = __builtin_amdgcn_rcpf(o[12]);
        om = u32x4{pk2(o[0] * il, o[1] * il), pk2(o[2] * il, o[3] * il), pk2(o[4] * il, o[5] * il), pk2(o[6] * il, o[7] * il)};
        const u32 r0 = pk2(o[8] * il, o[9] * il), r1 = pk2(o[10] * il, o[11] * il);
        orr = u32x4{r0, r1, (u32)__builtin_amdgcn_mov_dpp(r0, 0xf5, 0xf, 0xf, true), (u32)__builtin_amdgcn_mov_dpp(r1, 0xf5, 0xf, 0xf, true)};
        OPQV(om); OPQV(orr); };
      u64 bad = __builtin_amdgcn_ballot_w64(asu(o[12]) - 0x2b800000u >= 0x28000000u);
      OPQS(bad);
      fin();
      if (__builtin_expect(bad != 0, 0)) {
        const int gt = rb * 4 + qt, nv = gt + 1 < 9 ? gt + 1 : 9;
        if (hh) qr[0] = 0u;
        float m = -3.0e38f;
        for (int pass = 0; pass < 2; ++pass) {
          o = z16;
#pragma unroll 1
          for (int i = 0; i < nv; ++i) {
            const u32 d = (8 - i) * KVL;
            const u32 ma = (i & 7) ? 0u : ~0u, mb = i ? 0u : 0xff800000u;
            f32x16 x;
            for (int r = 0; r < 16; ++r) x[r] = asf((asu(cm[r]) & ma) ^ mb);
            x = mm32k8(lds_ld<u32x2>(at.aR + d), qr, mm32(lds_ld<u32x4>(at.aK + d), qm, x));
            if (pass == 0) { for (int r = 0; r < 16; ++r) m = fmaxf(m, x[r]); }
            else {
              for (int r = 0; r < 16; ++r) x[r] = __builtin_amdgcn_exp2f(x[r] - m);
              const u32x4 p0 = {pk2(x[0], x[1]), pk2(x[2], x[3]), pk2(x[4], x[5]), pk2(x[6], x[7])};
              const u32x4 p1 = {pk2(x[8], x[9]), pk2(x[10], x[11]), pk2(x[12], x[13]), pk2(x[14], x[15])};
              const u32x2 a = lds_tr(at.aV[0] + d), b = lds_tr(at.aV[1] + d), e = lds_tr(at.aV[2] + d), f = lds_tr(at.aV[3] + d);
              o = mm32(u32x4{a[0], a[1], b[0], b[1]}, p0, o); o = mm32(u32x4{e[0], e[1], f[0], f[1]}, p1, o);
            }
          }
          m = swap32_max(m);
        }
        fin();
      }
      STQ(6); LQA(3);
      st16(om, rs, at.o0, oso);
      st16(orr, rs, at.o1, oso);
      PIN();
      (void)__hip_atomic_fetch_add((LAS u32*)at.mb, inc, RLX_WG);
      sigx<0xff>(ws + gfl + (qt >> 1) * 8192 + (F_O + (s >> 3) + (qt & 1) * 2) * 128, at.l16 * 1024, vk<1>());
      STQ(7);
      if (isC) { STF(0, 1); STF(1, 2); STF(2, 3); STF(3, 24); STF(4, 4); } else { STF(11, 11); STF(12, 12); if (w >= 6) STF(9, 9); }
      STF(5, 5); STF(6, 6); STF(7, 7);
    }
    if (isC) {
      u32 res[10];
      auto ldres = [&](u32 xs, int l15, int g) {
        u32x4 a = ld16(rs, XE_A, xs), b = ld16(rs, XE_A + 2048, xs); u32x2 d = ld8(rs, XE_C + (g & 1) * 8, xs);
        res[0] = a[0]; res[1] = a[1]; res[2] = a[2]; res[3] = a[3]; res[4] = b[0]; res[5] = b[1]; res[6] = b[2]; res[7] = b[3]; res[8] = d[0]; res[9] = d[1]; };
      struct Ep { u32 xw, xr, fa, va, vc, vs, vq, one, xs, last, k[4]; int thr; u32x4 ones; GAS unsigned char* sg; };
      auto eset = [&](auto ki, Ep& e, const Ctx& c, int which) { constexpr int K = decltype(ki)::v;
        const u32 l16 = c.l16;
        if constexpr (K == 0) { SCAL();
          const u32 lx = which ? L_X2 : L_X1;
          e.last = which * layer; e.thr = eb + (which ? 85 : 28);
          e.k[0] = lx + wq * 5120; e.k[1] = lx + (wq ^ 1) * 5120; e.k[2] = L_FREE + MBW(wq ^ 1); e.k[3] = 40960 - cs * 4864 - (G & 1) * 128;
          e.xs = e.last ? G * 20480 + cs * 160 : xe_s + (layer * 2 + 1 + which) * XSZ; e.sg = ws + gfl + ((which ? 16 + F_X : F_X2) + half * 2 + (wq >> 1)) * 128;
          OPQS(e.last); OPQS(e.thr); OPQS(e.k[0]); OPQS(e.k[1]); OPQS(e.k[2]); OPQS(e.k[3]); OPQS(e.xs); OPQS(e.sg); }
        u32 t = e.last; OPQS(t);
        const bool fin = which && t;
        if constexpr (K == 1) { e.xr = l16 + e.k[0]; e.xw = l16 + e.k[1]; e.fa = e.k[2]; OPQV(e.xr); OPQV(e.xw); OPQV(e.fa); }
        if constexpr (K == 2) { e.va = (l16 & 240) * (fin ? 80 : 1); e.vc = l16 >> 8; OPQV(e.va); OPQV(e.vc); }
        if constexpr (K == 3) { e.vq = e.va + e.vc * (fin ? 8 : 256) + (fin ? 128 : 4096); e.va += e.vc * (fin ? 16 : 512); OPQV(e.va); OPQV(e.vq); }
        if constexpr (K == 4) { e.vs = (l16 >> 1 & 120) + e.k[3]; e.one = vk<OFF_END>(); OPQV(e.vs); }
        if constexpr (K == 5) { e.vs = __builtin_amdgcn_inverse_ballot_w64(0xf0000f0000f0000full) ? e.vs : e.one; OPQV(e.vs); }
        if constexpr (K == 6) { e.vc = (l16 & (fin ? 0 : 256)) ? e.one : e.vq; OPQV(e.vc); }
        if constexpr (K == 7) { e.vq = l16 * 512; e.one = vk<1>(); OPQV(e.vq); }
        if constexpr (K == 8) { e.ones = u32x4{vk<0x3f803f80u>(), vk<0x3f803f80u>(), vk<0x3f803f80u>(), vk<0x3f803f80u>()}; OPQV(e.ones); }
      };
      auto epi = [&](f32x4 (&acc)[5], const Ep& e, const Ctx& c, int which) {
        freed(c);
        const int t = lds_word(e.fa);
        f32x4 p[5];
        auto rd = [&]() { for (int j = 0; j < 5; ++j) { p[(j + 4) % 5] = lds_ld<f32x4>(e.xr + (j + 4) % 5 * 1024); PIN(); } };
        auto ap = [&](int j) { res[2 * j] = pk2(acc[j][0] + p[j][0], acc[j][1] + p[j][1]); res[2 * j + 1] = pk2(acc[j][2] + p[j][2], acc[j][3] + p[j][3]); };
        PIN();
        rd();
        const u64 m = pre(t, e.thr);
        PIN();
        ap(4); OPQV(res[8]); OPQV(res[9]);
        PIN();
        if (m != 0) { spin_ge<0>(e.fa, e.thr); rd(); ap(4); OPQV(res[8]); OPQV(res[9]); }
        if (!which) STQ(3);
        PIN();
        if (which && e.last) {
          const rsrc_t ro = __builtin_amdgcn_make_buffer_rsrc((void*)out, 0, 2048 * 1280, 0x00020000);
          __builtin_amdgcn_raw_buffer_store_b64(u32x2{res[8], res[9]}, ro, e.vc, e.xs, OAUX);
          ap(0); ap(1);
          const u32x4 o0 = {res[0], res[1], res[2], res[3]};
          __builtin_amdgcn_raw_buffer_store_b128(o0, ro, e.va, e.xs, OAUX);
          PIN();
          ap(2); ap(3);
          const u32x4 o1 = {res[4], res[5], res[6], res[7]};
          __builtin_amdgcn_raw_buffer_store_b128(o1, ro, e.va + 64, e.xs, OAUX);
          asm volatile("s_nop 1" :: "v"(o0), "v"(o1));
        } else {
          const f32x4 z4 = {};
          const s16x4 x2 = __builtin_bit_cast(s16x4, u32x2{res[8], res[9]});
          f32x4 sm = __builtin_amdgcn_mfma_f32_16x16x16bf16_1k(__builtin_bit_cast(s16x4, u32x2{e.ones[0], e.ones[1]}), x2, z4, 0, 0, 0);
          f32x4 sq = __builtin_amdgcn_mfma_f32_16x16x16bf16_1k(x2, x2, z4, 0, 0, 0);
          OPQV(sm); OPQV(sq);
          PIN();
          auto a = __builtin_amdgcn_permlane16_swap(res[8], asu(p[4][2]), false, false);
          auto b = __builtin_amdgcn_permlane16_swap(res[9], asu(p[4][3]), false, false);
          const u32x4 o2 = {a[0], b[0], a[1], b[1]};
          __builtin_amdgcn_raw_buffer_store_b128(o2, rs, e.vc, e.xs, SAUX);
          PIN();
          ap(0); ap(1);
          const u32x4 o0 = {res[0], res[1], res[2], res[3]};
          __builtin_amdgcn_raw_buffer_store_b128(o0, rs, e.va, e.xs, SAUX);
          sm = mm16p(e.ones, o0, sm); sq = mm16p(o0, o0, sq);
          PIN();
          ap(2); ap(3);
          const u32x4 o1 = {res[4], res[5], res[6], res[7]};
          sq = mm16p(o1, o1, sq); sm = mm16p(e.ones, o1, sm);
          PIN();
          __builtin_amdgcn_raw_buffer_store_b128(o1, rs, e.va + 2048, e.xs, SAUX);
          PIN();
          const bool b0 = __builtin_amdgcn_inverse_ballot_w64(0xaaaaaaaaaaaaaaaaull), b1 = __builtin_amdgcn_inverse_ballot_w64(0xccccccccccccccccull);
          const float d = b1 ? (b0 ? sq[3] : sq[2]) : (b0 ? sq[1] : sq[0]);
          __builtin_amdgcn_raw_buffer_store_b64(u32x2{asu(sm[0]), asu(d)}, rs, e.vs, e.xs, SAUX);
          asm volatile("" :: "v"(o0), "v"(o1), "v"(o2));
          PIN();
          freed(c);
          if (!which) STQ(4);
          sigx<0xffff>(e.sg, e.vq, e.one);
        }
      };
      {
        LANES();
        __builtin_amdgcn_s_setprio(CPRIO);
        const u32 kh = wq & 1, th = wq >> 1;
        ldres(xe_s + layer * 2 * XSZ, l15, g);
        Ctx k = c; k.l16 += kh * 5120; k.l16h += kh * 5120; k.l16t += kh * 5120;
        u32x4 B[10][2], W[50];
        int fv; u64 fm;
        auto wld = [&](auto ni) { constexpr int n = decltype(ni)::v;
          if constexpr (n < 50) {
            if constexpr (n == 35) chk(fm, c.mb, fb + 26);
            if constexpr (n == 34) fm = pre(fv, fb + 26);
            W[n] = far_ld<((8 + n / 5) % 15) * UN + (n % 5) * 1024, u32x4>(k);
            if constexpr (n % 5 == 4) freed(c);
            if constexpr (n == 29) fv = lds_word(c.mb);
          } };
        f32x4 acc[2][5]; const f32x4 z4 = {};
        for (int j = 0; j < 5; ++j) { acc[0][j] = f32x4{bflo(res[2 * j]), bfhi(res[2 * j]), bflo(res[2 * j + 1]), bfhi(res[2 * j + 1])}; OPQV(acc[0][j]); }
        Ep ep;
        sfor<0, 9>([&](auto i) { eset(i, ep, c, 0); });
        const u32 so = OFF_O + layer * OSZ + (rb * 8 + half * 4 + th * 2) * 20480 + kh * 10240;
        u32 se[2] = {so + kh * 20480, so + (kh ^ 1) * 20480}, vr = 8192 + g * 256 + l15 * 8, vb[4];
        for (int i = 0; i < 4; ++i) { vb[i] = c.l16 + ((2 * i + ROT * cs) & 7) * 1024; OPQV(vb[i]); }
        OPQV(vr); OPQS(se[0]); OPQS(se[1]);
        poll_ge<0>(ws, fl + (F_O + wq) * 128, 16);
        STQ(0); LQA(5);
        auto bld = [&](auto bi) { constexpr int b = decltype(bi)::v, st = b / 2, e = b % 2;
          if constexpr (st < 8) B[st][e] = ld16(rs, vb[st / 2] + st % 2 * 1024, se[e]);
          else { const u32x2 x = ld8(rs, vr + st % 2 * 1024, se[e]), y = ld8(rs, vr + st % 2 * 1024 + 128, se[e]); B[st][e] = u32x4{x[0], x[1], y[0], y[1]}; } };
        sfor<0, BLA>([&](auto i) { bld(i); PIN(); });
        spin_ge<0>(c.mb, fb + 23);
        sfor<0, WP>(wld);
        PIN();
        sfor<0, 45>([&](auto fi) {
          constexpr int f = decltype(fi)::v, st = f / 5, jn = f % 5, n = f + WP;
          mm16t(W[f], B[st][0], acc[0][jn]);
          wld(IC<n>{});
          PIN();
          if constexpr (st) mm16t(W[f], B[st][1], acc[1][jn]); else acc[1][jn] = mm16p(W[f], B[st][1], z4);
          if constexpr (f % 2 == 0 && BLA + f / 2 < 20) bld(IC<BLA + f / 2>{});
          if constexpr (f == 0) { STQ(1); LQA(6); }
          PIN();
        });
        for (int j = 0; j < 5; ++j) { acc[1][j] = mm16p(W[45 + j], B[9][1], acc[1][j]); PIN(); }
        for (int j = 0; j < 5; ++j) { acc[0][j] = mm16p(W[45 + j], B[9][0], acc[0][j]); lds_st<f32x4>(ep.xw + j * 1024, acc[1][j]); PIN(); }
        STQ(2); LQA(7);
        epi(acc[0], ep, c, 0);
        STQ(5);
        STF(0, 8); STF(1, 27); STF(2, 9); STF(3, 25); STF(4, 26); STF(5, 10);
      }
      {
        LANES();
        const u32 kh = wq & 1, th = wq >> 1;
        u32 xso = OFF_X + (layer * 2 + 1) * XSZ + (rb * 4 + wq) * XT;
        u32 hso = OFF_H + layer * HSZ + (rb * 8 + wq * 2) * HG + (s >> 3) * 27648 + ((s >> 1) & 3) * 1024, hs6 = (s & 7) < 6 ? hso + 24576 : OFF_END;
        u32 hvo = (l31 >> 4) * HG + (l15 + 16 * (2 * (s & 1) + hh)) * 16;
        const u32 bso = OFF_H + layer * HSZ + (rb * 8 + half * 4 + th * 2) * HG + kh * 27648;
        u32 bs0 = bso + kh * HG, bs1 = bso + (kh ^ 1) * HG, k5 = kh * 5120, rso = xe_s + (layer * 2 + 1) * XSZ;
        OPQV(c.l16); OPQV(hvo); OPQS(xso); OPQS(hso); OPQS(hs6); OPQS(bs0); OPQS(bs1); OPQS(k5); OPQS(rso);
        Ep ep; eset(IC<0>{}, ep, c, 1);
        poll_ge<0>(ws, fl + (F_X2 + wq) * 128, 16);
        STQ(0); LQA(9);
        u32x4 A[40], W[415], B[27][2];
        auto ald = [&](auto i) { constexpr int k = decltype(i)::v; OPQV(c.l16); A[k] = ld16(rs, c.l16 + (k & 3) * 1024, xso + (k >> 2) * 4096); };
        sfor<0, ALA>([&](auto i) { ald(i); PIN(); });
        Stat sa;
        for (int i = 0; i < 4; ++i) sa.sp[i] = ld8(rs, hh * 1024 + l31 * 8 + i * 256, xso + 40960);
        spin_ge<0>(c.mb, fb + 29);
        f32x16 acc[7];
        f32x4 ad[2][5];
        const f32x16 z16 = {}; const f32x4 z4 = {};
        sfor<0, GP>([&](auto i) { constexpr int f = decltype(i)::v; W[f] = far_ld<(30 + f) * 1024, u32x4>(c); });
        PIN();
        float k1, k2, ge[8], gp[8]; float& vv = sa.vv; u32& mo = sa.mo; u32 hp[4]; int fv; u64 fm; u32x2 cq;
        Ctx k;
        auto gop = [&](auto oi) { constexpr int o = PL.op[decltype(oi)::v], t = o / 128, kind = o % 128 / 8, j = o % 8;
          if constexpr (kind == 0) ge[j] = xmul(acc[t][j], acc[t][j]);
          if constexpr (kind == 1) ge[j] = xfma(k2, ge[j], k1);
          if constexpr (kind == 2) ge[j] = xmul(acc[t][j], ge[j]);
          if constexpr (kind == 3) ge[j] = xexp(ge[j]);
          if constexpr (kind == 4) gp[j] = xmul(acc[t][j], acc[t][8 + j]);
          if constexpr (kind == 5) ge[j] = xfma(ge[j], vv, vv);
          if constexpr (kind == 6) ge[j] = xrcp(ge[j]);
          if constexpr (kind == 7) gp[j] = xmul(gp[j], ge[j]);
          if constexpr (kind == 8) hp[j] = xpk(gp[2 * j], gp[2 * j + 1]);
          if constexpr (kind == 9) st16(u32x4{hp[0], hp[1], hp[2], hp[3]}, rs, hvo, t < 6 ? hso + t * 4096 : hs6); };
        auto side = [&](auto ti) {
          constexpr int tau = decltype(ti)::v;
          if constexpr (sig_at(tau) >= 0) WAITV(vm_after(PL.st[EV_TILE[sig_at(tau)]], tau));
          if constexpr (hb_at(tau) >= 0) { constexpr int b = hb_at(tau), st = b / 2, e = b % 2;
            OPQV(c.l16); B[st][e] = ld16(rs, c.l16 + (st & 3) * 1024, (e ? bs1 : bs0) + (st >> 2) * 4096); }
          if constexpr (tau == T_RES) ldres(rso, (c.l16 >> 4) & 15, c.l16 >> 8);
        };
        auto ahead = [&](auto ti) { constexpr int tau = decltype(ti)::v, n = tau + GP;
          if constexpr (ck_at(n) >= 0) chk(fm, c.mb, fb + ck_thr(ck_at(n)));
          if constexpr (ck_at(n + 1) >= 0) fm = pre(fv, fb + ck_thr(ck_at(n + 1)));
          if constexpr (n < 280) W[n] = far_ld<((30 + n) % 150) * 1024, u32x4>(c);
          else if constexpr (n < 415) W[n] = far_ld<((1 + (n - 280) / 5) % 15) * UN + (n % 5) * 1024, u32x4>(k);
          if constexpr (fr_at(n)) freed(c);
          if constexpr (ck_at(n + 6) >= 0) fv = lds_word(c.mb); };
        auto both = [&](auto ti) { constexpr int tau = decltype(ti)::v;
          constexpr bool w = tau % WEV == WEV - 1 && tau + WEV < 415, bb = tau >= 279 && tau < 414 && tau % 5 == 4;
          if constexpr (w && bb) usev2(W[tau + WEV], B[(tau - 279) / 5][1]); else if constexpr (w) usev(W[tau + WEV]); else if constexpr (bb) usev(B[(tau - 279) / 5][1]); };
        sfor<0, 280>([&](auto fi) {
          constexpr int f = decltype(fi)::v, t = gu_tile(f), ks = gu_ks(f);
          if constexpr (ks == 0 && t >= 3) acc[t] = mm32k8p(cq, (t & 1) ? u32x2{0u, mo} : u32x2{mo, 0u}, z16);
          if constexpr (ks == 0 && t < 3) acc[t] = mm32p(W[f], A[ks], z16); else acc[t] = mm32p(W[f], A[ks], acc[t]);
          if constexpr (f == 238) { k = c; k.l16 += k5; k.l16h += k5; k.l16t += k5; OPQV(k.l16); OPQV(k.l16h); OPQV(k.l16t); }
          ahead(fi);
          if constexpr (ag_at(f) >= 0) ald(IC<ag_at(f)>{});
          if constexpr (st_at(f) >= 0) stat<st_at(f)>(sa);
          if constexpr (st_at(f) == 7) { k1 = -2.f * 0.7978845608028654f * 1.4426950408889634f * sa.rstd; k2 = k1 * 0.044715f * sa.rstd * sa.rstd; OPQV(k1); OPQV(k2); }
          if constexpr (f == 61) acc[0] = mm32k8p(cq, u32x2{mo, 0u}, acc[0]);
          if constexpr (f == 62) acc[1] = mm32k8p(cq, u32x2{0u, mo}, acc[1]);
          if constexpr (f == 69) acc[2] = mm32k8p(cq, u32x2{mo, 0u}, acc[2]);
          if constexpr (cq_at(f) >= 0) cq = far_ld<L_CS + 1024 + cq_at(f), u32x2>(c);
          side(fi);
          sfor<PL.first[2 * f], PL.first[2 * f + 2]>(gop);
          both(fi);
          if constexpr (dg_at(f) >= 0) STQ(dg_at(f));
          if constexpr (ld_at(f) >= 0) LQ(ld_at(f));
          if constexpr (f == 0 || f == 45) LQ(10 + f / 45);
          PIN();
        });
        float rf[4];
        sfor<280, 410>([&](auto fi) {
          constexpr int f = decltype(fi)::v, st = (f - 280) / 5, jn = (f - 280) % 5;
          if constexpr (st) mm16t(W[f], B[st][0], ad[0][jn]); else ad[0][jn] = mm16p(W[f], B[st][0], z4);
          if constexpr (f >= T_ADD && f < T_ADD + 5) { constexpr int j = (jn + 3) % 5; rf[0] = xlo(res[2 * j]); rf[1] = xhi(res[2 * j]); rf[2] = xlo(res[2 * j + 1]); rf[3] = xhi(res[2 * j + 1]); }
          ahead(fi);
          side(fi);
          sfor<PL.first[2 * f], PL.first[2 * f + 1]>(gop);
          PIN();
          if constexpr (st) mm16t(W[f], B[st][1], ad[1][jn]); else ad[1][jn] = mm16p(W[f], B[st][1], z4);
          if constexpr (f >= T_ADD && f < T_ADD + 5) { f32x4& x = ad[0][(jn + 3) % 5]; PIN(); x += f32x4{rf[0], rf[1], rf[2], rf[3]}; OPQV(x); }
          if constexpr (f > T_EP && f < T_EP + 9) eset(IC<f - T_EP>{}, ep, c, 1);
          sfor<PL.first[2 * f + 1], PL.first[2 * f + 2]>(gop);
          both(fi);
          if constexpr (dg_at(f) >= 0) STQ(dg_at(f));
          if constexpr (ld_at(f) >= 0) LQ(ld_at(f));
          PIN();
        });
        for (int j = 0; j < 5; ++j) { ad[1][j] = mm16p(W[410 + j], B[26][1], ad[1][j]); PIN(); }
        for (int j = 0; j < 5; ++j) { ad[0][j] = mm16p(W[410 + j], B[26][0], ad[0][j]); lds_st<f32x4>(ep.xw + j * 1024, ad[1][j]); PIN(); }
        if constexpr (dg_at(414) >= 0) STQ(dg_at(414));
        if (!ep.last) LQ(4); else LQ(8);
        epi(ad[0], ep, c, 1);
        STF(0, 11); STF(1, 16); STF(2, 17); STF(3, 18); STF(4, 19); STF(5, 20); STF(6, 21); STF(7, 12); STF(8, 22); STF(9, 13); STF(10, 23); STF(11, 14); if (ep.last) ST(15);
      }
    } else {
      LANES();
      if (wq < 2) {
        STR_INIT(23);
        __builtin_amdgcn_s_setprio(HPRIO);
        const u32 og = OFF_WG + s * 286720, od = OFF_WD + cs * 276480;
        if ((EB & 4) && !layer) { poll_tag(ws, OFF_SLOT + (rb * 16 + s) * 1024 + 512 + l31 * 8, tag); t.seq -= 28; stream<57>(t, OFF_WO + cs * 102400, 8, 7, eb + 15, 0); }
        stream<NC>(t, OFF_WO + cs * 102400 + 7 * UN, 0, 3, eb + 17, 0);
        stream<NC>(t, og, 3, 3, eb + 17, 0);
        stream<NC>(t, og + 5 * UN, 8, 1, eb + WPAUSE, 1);
        stream<NC>(t, og + 6 * UN, 9, WN - 1, eb + 19, 1);
        stream<NC>(t, og + 3 * UN, 6, 2, eb + 29, 0);
        stream<NC>(t, og + (5 + WN) * UN, 8 + WN, 10 - WN, eb + RP, 0);
        STH(16);
        stream<NB>(t, og + 15 * UN, 3, 1, eb + 30 + GPAUSE, 1);
        stream<NB>(t, og + 16 * UN, 4, 12, eb + 31, 1);
        STH(17);
        stream<NB>(t, od, 1, 27, eb + 43, 1);
        STH(18);
        if (layer == 0) {
          stream<NC>(t, OFF_WQ + s * 163840, 0, 1, QPAUSE, 1);
          stream<NC>(t, OFF_WQ + s * 163840 + UN, 1, 12, 73, 1);
          stream<NC>(t, OFF_WQ + s * 163840 + 13 * UN, 13, 2, 86, 0);
        }
        __builtin_amdgcn_s_setprio(0);
        STH(19);
      } else {
        __builtin_amdgcn_s_setprio(0);
        const u32 fa = L_FREE + MBW(lane) + (lane & 3) * 4;
        const u32 pa = L_FULL + MBW(g) + (wq & 1) * 64 + l15 * 4;
        if (wq == 2) {
          constexpr unsigned long long TB = (T_EV(0) - 1) | (T_EV(1) - 1) << 8 | (T_EV(2) - 1) << 16 | (T_EV(3) - 1) << 24 | (unsigned long long)(T_EV(4) - 1) << 32 | (unsigned long long)(LS + T_EV(0) - 1) << 40;
          GAS u32* p = (GAS u32*)(ws + fl + F_H * 128 + (g & 1) * HLS);
          if (PFM & 2) { spin_ge<2>(fa, eb + PF1); pull<15 * UN, 28 * UN, 4>(rs, OFF_WG + s * 286720, rb & 3, c.l16); }
          if (PFM & 4) { spin_ge<2>(fa, eb + PF2); pull<0, 27 * UN, 8>(rs, OFF_WD + cs * 276480, (rb & 3) * 2 + half, c.l16); }
          spin_ge<2>(fa, eb + fr_cnt(PL.sig[0] + 1 + GP) + PDLY);
          ST(22);
          int n = POLL_MAX, mo = 0; (void)mo;
#pragma unroll 1
          for (; n > 0; --n) {
            u32 h0, h1;
            if (SPOLL & 2) { asm volatile("s_load_dword %0, %2, %3 glc\n\ts_load_dword %1, %2, %4 glc\n\ts_waitcnt lgkmcnt(0)" : "=&s"(h0), "=&s"(h1) : "s"(ws), "s"(fl + F_H * 128), "s"(fl + F_H * 128 + HLS) : "memory"); h0 = (g & 1) ? h1 : h0; }
            else h0 = __hip_atomic_load(p, RLX_AGENT);
            const int m = __builtin_popcount(h0 & 0x88888u);
            lds_post(pa, fb + (int)((TB >> (8 * m)) & 255));
            if (__builtin_amdgcn_ballot_w64(m < 5) == 0) break;
            __builtin_amdgcn_s_sleep(PSLP);
          }
          if (n == 0) lds_post(pa, fb + LS + T_EV(0) - 1);
          ST(23);
        }
        if (wq == 3) {
          if (!layer) poll_tag(ws, OFF_SLOT + (rb * 16 + s) * 1024 + 512 + l31 * 8, tag);
          if (!layer) for (int n = 0; n < 2; ++n) { u32x4 d; float cv[32];
            for (int i = 0; i < 32; ++i) cv[i] = ld4(rs, 768 + n * 256 + (i >> 3) * 64 + (g & 1) * 512 + l15 * 4 + hh * 2048, OFF_CS + s * 32768 + (i & 7) * 4096);
            PIN();
            for (int j = 0; j < 4; ++j) { float v = 0;
              for (int i = 0; i < 8; ++i) v += cv[j * 8 + i];
              const u32 pp = split2(swap32_add(v)), x = hh ? pp >> 16 : pp & 0xffffu; d[j] = (n * 4 + j < 7) ? x | (x << 16) : 0u; }
            lds_st<u32x4>(L_CS + 1024 + n * 1024 + c.l16, d); }
          lds_post(pa, fb + T_X2 - 1);
          GAS u32* p = (GAS u32*)(ws + gfl + F_H * 128 + (s >> 3) * HLS + l15 * 8192);
          sfor<0, 5>([&](auto ei) { constexpr int e = decltype(ei)::v;
            spin_ge<1>(fa, eb + fr_cnt(PL.sig[e] + 1 + GP));
            if (lane < 16) (void)__hip_atomic_fetch_add(p, vk<1u << (4 * e)>(), RLX_AGENT);
          });
        }
        if ((PFM & 8) && wq == 3 && !layer) pull<0, 16 * UN, 4>(rs, OFF_WQ + s * 163840, rb & 3, c.l16);
        if (wq == 3) { spin_ge<2>(fa, eb + 71); lds_post(pa, fb + LS + T_KV - 1); }
      }
    }
  }
  if (EPOCH && blockIdx.x == 0 && threadIdx.x == 0) put_tag(ws, OFF_EP, tag);
  WAITV(0);
}

#if defined(__HIP_DEVICE_COMPILE__)
#pragma clang attribute pop
#endif
#define WSOFF 0
extern "C" void kernel_launch(void* const* d_in, const int* in_sizes, int n_in, void* d_out, int out_size, void* d_ws, size_t ws_size, hipStream_t stream) {
  (void)in_sizes; (void)n_in; (void)out_size; (void)ws_size;
  (void)hipFuncSetAttribute((const void*)mega, hipFuncAttributeMaxDynamicSharedMemorySize, LDS_BYTES);
  if (!EPOCH) (void)hipMemsetAsync(d_ws, 0, 262144, stream);
  mega<<<256, 512, LDS_BYTES, stream>>>((cw_t)d_in[2], (cw_t)d_in[1], (cw_t)d_in[0], (cw_t)d_in[4], (cw_t)d_in[5], (cw_t)d_in[6], (unsigned short*)d_out, (unsigned char*)d_ws + WSOFF);
}
```
